```python
import torch

def _softmax(x, axis=-1):
    return torch.softmax(x, dim=axis)
def _gelu(x):
    return torch.nn.functional.gelu(x, approximate="tanh")
def _silu(x):
    return torch.nn.functional.silu(x)
def _sigmoid(x):
    return torch.sigmoid(x)
def _layernorm(x, axis=-1, eps=1e-5):
    m = x.mean(dim=axis, keepdim=True)
    v = x.var(dim=axis, keepdim=True, unbiased=False)
    return (x - m) / torch.sqrt(v + eps)
def _scatter_add(tgt, idx, src, axis=0):
    return tgt.clone().index_add_(axis, idx, src)
def _segment_sum(src, seg, n):
    out = torch.zeros((n,) + src.shape[1:], dtype=src.dtype)
    return out.index_add_(0, seg, src)
def _pad(x, pw):
    flat = tuple(v for pair in reversed(pw) for v in pair)
    return torch.nn.functional.pad(x, flat)

kernel_name = 'standalone__gqa_block_cached__near_ridge'

def setup_inputs():

    _w = (lambda a: (lambda t: t.to(torch.bfloat16) if t.is_floating_point() else t)(torch.as_tensor(a)))
    B, Sq, D = 8, 112, 640
    Hq, Hkv, Sk, Dh = 32, 32, 512, 128
    assert Hq % Hkv == 0, (Hq, Hkv)
    return {
        "x": _w((lambda *s: torch.randn(*s, dtype=torch.bfloat16) * 0.8)(B, Sq, D)),
        "k_cache": _w((lambda *s: torch.randn(*s, dtype=torch.bfloat16) * 0.8)(B, Hkv, Sk, Dh)),
        "v_cache": _w((lambda *s: torch.randn(*s, dtype=torch.bfloat16) * 0.05)(B, Hkv, Sk, Dh)),
        "wq": _w((lambda *s: torch.randn(*s, dtype=torch.bfloat16) * 0.8)(D, Hq * Dh) * (D ** -0.5)),
        "wo": _w((lambda *s: torch.randn(*s, dtype=torch.bfloat16) * 0.05)(Hq * Dh, D) * ((Hq * Dh) ** -0.5)),
    }

def reference(x, k_cache, v_cache, wq, wo):
    B, Sq, D = x.shape
    Hq, Dh = 32, 128
    Hkv = k_cache.shape[1]
    rep = Hq // Hkv
    q = (lambda x, *d: x.permute(*d))((x @ wq).reshape(B, Sq, Hq, Dh), 0, 2, 1, 3)
    k = (lambda x, n, axis: torch.repeat_interleave(x, n, dim=axis))(k_cache, rep, axis=1)
    v = (lambda x, n, axis: torch.repeat_interleave(x, n, dim=axis))(v_cache, rep, axis=1)
    scores = torch.einsum("bhqd,bhkd->bhqk", q, k) * (Dh ** -0.5)
    p = _softmax(scores, axis=-1)
    o = torch.einsum("bhqk,bhkd->bhqd", p, v)
    return (lambda x, *d: x.permute(*d))(o, 0, 2, 1, 3).reshape(B, Sq, Hq * Dh) @ wo
```

```cpp
#include <hip/hip_runtime.h>
#include <hip/hip_bf16.h>
#include <stdint.h>

namespace {

using bf16x8 = __attribute__((ext_vector_type(8))) short;
using s16x4 = __attribute__((ext_vector_type(4))) short;
using f32x16 = __attribute__((ext_vector_type(16))) float;
using f32x4 = __attribute__((ext_vector_type(4))) float;
using f32x2 = __attribute__((ext_vector_type(2))) float;
using u32x4 = __attribute__((ext_vector_type(4))) unsigned;
using u32x2 = __attribute__((ext_vector_type(2))) unsigned;
typedef short v4i16_t __attribute__((ext_vector_type(4)));
typedef __attribute__((address_space(3))) char* lds_ptr;
typedef __attribute__((address_space(3))) const char* lds_cptr;

#define DI __device__ __forceinline__
#define MFMA32(a, b, c) __builtin_amdgcn_mfma_f32_32x32x16_bf16((a), (b), (c), 0, 0, 0)
#define MFMA16(a, b, c) __builtin_amdgcn_mfma_f32_16x16x32_bf16((a), (b), (c), 0, 0, 0)

constexpr int NB = 8, SQ = 112, DM = 640, NH = 32, SK = 512, DH = 128;
constexpr int SLOT = 16384;
constexpr int LDS1 = 10 * SLOT;
constexpr float SCL = 0.08838834764831845f * 1.4426950408889634f;

constexpr size_t WS_CNT = 0;
constexpr size_t WS_O = 4096;
constexpr size_t WS_O_BYTES = (size_t)NB * 128 * 7 * 1024;
constexpr size_t WS_WOT = WS_O + WS_O_BYTES;
constexpr size_t WS_WOT_BYTES = (size_t)8 * 128 * 5 * 1024;
constexpr size_t WS_PART = WS_WOT + WS_WOT_BYTES;
constexpr size_t WS_PART_BYTES = (size_t)64 * 4 * 2240 * 16;

DI s16x4 vtr(lds_cptr p) {
  return __builtin_bit_cast(s16x4, __builtin_amdgcn_ds_read_tr16_b64_v4i16((__attribute__((address_space(3))) v4i16_t*)p));
}
DI bf16x8 ldsr128(lds_cptr p) { return *(const __attribute__((address_space(3))) bf16x8*)p; }
DI bf16x8 cat8(s16x4 lo, s16x4 hi) { return __builtin_shufflevector(lo, hi, 0, 1, 2, 3, 4, 5, 6, 7); }
typedef __bf16 bfv2 __attribute__((ext_vector_type(2)));
DI unsigned cvtpk(float lo, float hi) {
  const f32x2 v = {lo, hi};
  return __builtin_bit_cast(unsigned, __builtin_convertvector(v, bfv2));
}
template <int S>
DI bf16x8 pack8(const f32x16& x) {
  u32x4 p = {cvtpk(x[8 * S + 0], x[8 * S + 1]), cvtpk(x[8 * S + 2], x[8 * S + 3]), cvtpk(x[8 * S + 4], x[8 * S + 5]),
             cvtpk(x[8 * S + 6], x[8 * S + 7])};
  return __builtin_bit_cast(bf16x8, p);
}
DI void glds16(const void* sbase, unsigned voff, unsigned lds_dst) {
  unsigned sv;
  asm volatile("s_mov_b32 %0, m0\n\ts_mov_b32 m0, %3\n\ts_nop 2\n\tglobal_load_lds_dwordx4 %1, %2\n\ts_mov_b32 m0, %0"
               : "=&s"(sv)
               : "v"(voff), "s"(sbase), "s"(lds_dst)
               : "memory");
}
template <int N>
DI void wait_bar() {
  asm volatile("s_waitcnt vmcnt(%0) lgkmcnt(0)\n\ts_barrier" ::"i"(N) : "memory");
}
DI void lds_bar() { asm volatile("s_waitcnt lgkmcnt(0)\n\ts_barrier" ::: "memory"); }

constexpr int NITEM = 26;
constexpr int ORDER[NITEM] = {0, 1, 10, 2, 11, 3, 12, 4, 5, 13, 6, 7, 8, 9, 14, 15, 16, 17, 18, 19, 20, 21, 22, 23, 24, 25};
constexpr int POINT[NITEM] = {0, 0, 0, 0, 0, 1, 1, 2, 3, 3, 4, 5, 6, 7, 8, 8, 9, 9, 10, 10, 11, 12, 13, 14, 15, 16};
constexpr int isize(int it) { return it < 10 ? 4 : 2; }
constexpr int after(int it, int pt) {
  int n = 0;
  bool seen = false;
  for (int i = 0; i < NITEM; ++i) {
    if (seen && POINT[i] <= pt) n += isize(ORDER[i]);
    if (ORDER[i] == it) seen = true;
  }
  return n;
}
constexpr int kslot(int t) { return t < 4 ? 6 + t : t - 2; }
constexpr int vslot(int t) { return t < 2 ? t : (t < 6 ? t + 4 : t - 4); }

struct K1Ctx {
  const char* xb;
  const char* wqb;
  const char* kb;
  const char* vb;
  unsigned vx0, vx1, vw0, vw1, vk0, vk1, vv0, vv1;
  unsigned ldsw;
};

template <int IT>
DI void issue_item(const K1Ctx& c) {
  if constexpr (IT < 10) {
    constexpr int sl = 2 * (IT % 3);
    glds16(c.xb + IT * 128, c.vx0, c.ldsw + sl * SLOT);
    glds16(c.xb + IT * 128, c.vx1, c.ldsw + sl * SLOT + 1024);
    glds16(c.wqb + (size_t)IT * 64 * 8192, c.vw0, c.ldsw + (sl + 1) * SLOT);
    glds16(c.wqb + (size_t)IT * 64 * 8192, c.vw1, c.ldsw + (sl + 1) * SLOT + 1024);
  } else if constexpr (IT < 18) {
    constexpr int t = IT - 10;
    glds16(c.kb + t * 16384, c.vk0, c.ldsw + kslot(t) * SLOT);
    glds16(c.kb + t * 16384, c.vk1, c.ldsw + kslot(t) * SLOT + 1024);
  } else {
    constexpr int t = IT - 18;
    glds16(c.vb + t * 16384, c.vv0, c.ldsw + vslot(t) * SLOT);
    glds16(c.vb + t * 16384, c.vv1, c.ldsw + vslot(t) * SLOT + 1024);
  }
}
template <int PT, int I = 0>
DI void issue_point(const K1Ctx& c) {
  if constexpr (I < NITEM) {
    if constexpr (POINT[I] == PT) issue_item<ORDER[I]>(c);
    issue_point<PT, I + 1>(c);
  }
}

template <int I>
DI void proj_stage(const K1Ctx& c, lds_cptr lds, unsigned xoff, unsigned woff0, unsigned woff1, f32x16& q0, f32x16& q1) {
  wait_bar<after(I, I - 1 < 0 ? 0 : I - 1)>();
  if constexpr (I >= 1) issue_point<I>(c);
  constexpr int sl = 2 * (I % 3);
  lds_cptr xs = lds + sl * SLOT, ws = lds + (sl + 1) * SLOT;
#pragma unroll
  for (int ks = 0; ks < 4; ++ks) {
    const bf16x8 xf = ldsr128(xs + (xoff ^ (unsigned)(ks * 32)));
    const bf16x8 a0 = cat8(vtr(ws + woff0 + ks * 4096), vtr(ws + woff0 + ks * 4096 + 1024));
    const bf16x8 a1 = cat8(vtr(ws + woff1 + ks * 4096), vtr(ws + woff1 + ks * 4096 + 1024));
    q0 = MFMA32(a0, xf, q0);
    q1 = MFMA32(a1, xf, q1);
  }
}

template <int T>
DI void qk_step(const K1Ctx& c, lds_cptr lds, unsigned koff, const bf16x8 (&qf)[8], f32x16& s) {
  wait_bar<after(10 + T, 10 + T - 1)>();
  if constexpr (T <= 6) issue_point<10 + T>(c);
  lds_cptr kp = lds + kslot(T) * SLOT;
  s = f32x16{};
#pragma unroll
  for (int ks = 0; ks < 8; ++ks) s = MFMA32(ldsr128(kp + (koff ^ (unsigned)(ks * 32))), qf[ks], s);
}

template <int T>
DI void pv_step(lds_cptr lds, const unsigned (&voff)[4], const bf16x8& p0, const bf16x8& p1, f32x16 (&o)[4]) {
  wait_bar<after(18 + T, 16)>();
  lds_cptr vp = lds + vslot(T) * SLOT;
#pragma unroll
  for (int dt = 0; dt < 4; ++dt) {
    const bf16x8 a0 = cat8(vtr(vp + voff[dt]), vtr(vp + voff[dt] + 2048));
    const bf16x8 a1 = cat8(vtr(vp + voff[dt] + 4096), vtr(vp + voff[dt] + 4096 + 2048));
    o[dt] = MFMA32(a0, p0, o[dt]);
    o[dt] = MFMA32(a1, p1, o[dt]);
  }
}

DI float max16(const f32x16& s) {
  float a = fmaxf(fmaxf(s[0], s[1]), s[2]), b = fmaxf(fmaxf(s[3], s[4]), s[5]);
  a = fmaxf(fmaxf(a, s[6]), s[7]);
  b = fmaxf(fmaxf(b, s[8]), s[9]);
  a = fmaxf(fmaxf(a, s[10]), s[11]);
  b = fmaxf(fmaxf(b, s[12]), s[13]);
  a = fmaxf(fmaxf(a, s[14]), s[15]);
  return fmaxf(a, b);
}
DI float swap32(float v) { return __shfl_xor(v, 32); }

__global__ __launch_bounds__(512, 2) void k1_attn(const char* __restrict__ x, const char* __restrict__ kc,
                                                   const char* __restrict__ vc, const char* __restrict__ wq,
                                                   const unsigned short* __restrict__ wo, char* __restrict__ ws) {
  extern __shared__ __attribute__((aligned(16))) char lds_raw[];
  const lds_ptr lds = (lds_ptr)lds_raw;
  const int tid = threadIdx.x, lane = tid & 63;
  const int w = __builtin_amdgcn_readfirstlane(tid >> 6);
  const int qt = w & 3, g = w >> 2;
  const int bid = blockIdx.x, xcd = bid & 7, idx = bid >> 3;
  const int h = xcd * 4 + (idx & 3), b = idx >> 2;
  const int r32 = lane & 31, hh = lane >> 5, blk = (lane >> 4) & 1, q4 = (lane & 15) >> 2, p = lane & 3;

  if (bid == 0 && tid < 64) *(unsigned*)(ws + WS_CNT + tid * 64) = 0u;

  K1Ctx c;
  c.xb = x + (size_t)b * SQ * DM * 2;
  c.wqb = wq + h * 256;
  c.kb = kc + (size_t)(b * NH + h) * SK * DH * 2;
  c.vb = vc + (size_t)(b * NH + h) * SK * DH * 2;
  {
    const int xr0 = 16 * w + (lane >> 3), xr1 = xr0 + 8;
    c.vx0 = (unsigned)(min(xr0, SQ - 1) * 1280 + (((lane & 7) ^ ((xr0 >> 1) & 7)) << 4));
    c.vx1 = (unsigned)(min(xr1, SQ - 1) * 1280 + (((lane & 7) ^ ((xr1 >> 1) & 7)) << 4));
    const int r0 = 8 * w + (lane >> 4), r1 = r0 + 4;
    c.vw0 = (unsigned)(r0 * 8192 + (((lane & 15) ^ ((r0 & 3) << 2)) << 4));
    c.vw1 = (unsigned)(r1 * 8192 + (((lane & 15) ^ ((r1 & 3) << 2)) << 4));
    c.vk0 = (unsigned)(r0 * 256 + (((lane & 15) ^ (r0 & 15)) << 4));
    c.vk1 = (unsigned)(r1 * 256 + (((lane & 15) ^ (r1 & 15)) << 4));
    c.vv0 = (unsigned)(r0 * 256 + (((lane & 15) ^ ((r0 & 3) << 2)) << 4));
    c.vv1 = (unsigned)(r1 * 256 + (((lane & 15) ^ ((r1 & 3) << 2)) << 4));
  }
  c.ldsw = (unsigned)(uintptr_t)lds_raw + (unsigned)w * 2048u;
  issue_point<0>(c);

  f32x16 q0 = {}, q1 = {};
  {
    const int xrow = 32 * qt + r32;
    const unsigned xoff = (unsigned)(xrow * 128 + ((hh ^ ((xrow >> 1) & 7)) << 4));
    const int sp = ((p & 1) << 1) | (p >> 1);
    const int d0 = 2 * g, d1 = 2 * g + 1;
    const unsigned wrow = (unsigned)((8 * hh + q4) * 256 + 8 * (sp & 1));
    const unsigned woff0 = wrow + (unsigned)((4 * (d0 ^ q4) + 2 * blk + (sp >> 1)) << 4);
    const unsigned woff1 = wrow + (unsigned)((4 * (d1 ^ q4) + 2 * blk + (sp >> 1)) << 4);
    proj_stage<0>(c, lds, xoff, woff0, woff1, q0, q1);
    proj_stage<1>(c, lds, xoff, woff0, woff1, q0, q1);
    proj_stage<2>(c, lds, xoff, woff0, woff1, q0, q1);
    proj_stage<3>(c, lds, xoff, woff0, woff1, q0, q1);
    proj_stage<4>(c, lds, xoff, woff0, woff1, q0, q1);
    proj_stage<5>(c, lds, xoff, woff0, woff1, q0, q1);
    proj_stage<6>(c, lds, xoff, woff0, woff1, q0, q1);
    proj_stage<7>(c, lds, xoff, woff0, woff1, q0, q1);
    proj_stage<8>(c, lds, xoff, woff0, woff1, q0, q1);
    proj_stage<9>(c, lds, xoff, woff0, woff1, q0, q1);
  }
  bf16x8 qf[8];
  lds_bar();
  {
    lds_ptr e = lds + qt * 8192 + g * 4096 + lane * 16;
    *(__attribute__((address_space(3))) bf16x8*)(e) = pack8<0>(q0);
    *(__attribute__((address_space(3))) bf16x8*)(e + 1024) = pack8<1>(q0);
    *(__attribute__((address_space(3))) bf16x8*)(e + 2048) = pack8<0>(q1);
    *(__attribute__((address_space(3))) bf16x8*)(e + 3072) = pack8<1>(q1);
  }
  lds_bar();
  {
    lds_cptr e = lds + qt * 8192 + lane * 16;
#pragma unroll
    for (int ks = 0; ks < 8; ++ks) qf[ks] = ldsr128(e + ks * 1024);
  }
  f32x16 s[8];
  {
    const int krow = 32 * g + r32;
    const unsigned koff = (unsigned)(krow * 256 + ((hh ^ (krow & 15)) << 4));
    qk_step<0>(c, lds, koff, qf, s[0]);
    qk_step<1>(c, lds, koff, qf, s[1]);
    qk_step<2>(c, lds, koff, qf, s[2]);
    qk_step<3>(c, lds, koff, qf, s[3]);
    qk_step<4>(c, lds, koff, qf, s[4]);
    qk_step<5>(c, lds, koff, qf, s[5]);
    qk_step<6>(c, lds, koff, qf, s[6]);
    qk_step<7>(c, lds, koff, qf, s[7]);
  }
  float m = max16(s[0]);
#pragma unroll
  for (int t = 1; t < 8; ++t) m = fmaxf(m, max16(s[t]));
  m = fmaxf(m, swap32(m));
  const float nmc = -m * SCL;
  float l = 0.f;
  bf16x8 pf[8][2];
#pragma unroll
  for (int t = 0; t < 8; ++t) {
#pragma unroll
    for (int r = 0; r < 16; ++r) {
      s[t][r] = __builtin_amdgcn_exp2f(__builtin_fmaf(s[t][r], SCL, nmc));
      l += s[t][r];
    }
    pf[t][0] = pack8<0>(s[t]);
    pf[t][1] = pack8<1>(s[t]);
  }
  l += swap32(l);

  if (w < 5) {
#pragma unroll
    for (int i = 0; i < 4; ++i) {
      const int f = bid * 20 + w * 4 + i, kb = f / 40, nb = f % 40;
      const int gg = lane >> 4;
      const int k0 = 32 * kb + 16 * (gg & 1) + 4 * (gg >> 1);
      const unsigned short* src = wo + (size_t)k0 * DM + 16 * nb + (lane & 15);
      unsigned short e[8];
#pragma unroll
      for (int j = 0; j < 8; ++j) e[j] = src[(8 * (j >> 2) + (j & 3)) * DM];
      u32x4 v = {(unsigned)e[0] | ((unsigned)e[1] << 16), (unsigned)e[2] | ((unsigned)e[3] << 16),
                 (unsigned)e[4] | ((unsigned)e[5] << 16), (unsigned)e[6] | ((unsigned)e[7] << 16)};
      *(u32x4*)(ws + WS_WOT + ((size_t)((nb / 5) * 128 + kb) * 5 + (nb % 5)) * 1024 + lane * 16) = v;
    }
  }

  f32x16 o[4] = {};
  {
    unsigned voff[4];
    const unsigned vrow = (unsigned)((32 * g + 4 * hh + q4) * 256 + 8 * (p & 1));
#pragma unroll
    for (int dt = 0; dt < 4; ++dt) voff[dt] = vrow + (unsigned)((4 * (dt ^ q4) + 2 * blk + (p >> 1)) << 4);
    pv_step<0>(lds, voff, pf[0][0], pf[0][1], o);
    pv_step<1>(lds, voff, pf[1][0], pf[1][1], o);
    pv_step<2>(lds, voff, pf[2][0], pf[2][1], o);
    pv_step<3>(lds, voff, pf[3][0], pf[3][1], o);
    pv_step<4>(lds, voff, pf[4][0], pf[4][1], o);
    pv_step<5>(lds, voff, pf[5][0], pf[5][1], o);
    pv_step<6>(lds, voff, pf[6][0], pf[6][1], o);
    pv_step<7>(lds, voff, pf[7][0], pf[7][1], o);
  }
  {
    const int mslot = (w >> 1) < 2 ? 4 + (w >> 1) : (w >> 1) - 2;
    lds_ptr mb = lds + mslot * SLOT + (w & 1) * 8192 + lane * 16;
    f32x16 sa, sb, ka, kb2;
    if (g == 0) { sa = o[2]; sb = o[3]; ka = o[0]; kb2 = o[1]; }
    else        { sa = o[0]; sb = o[1]; ka = o[2]; kb2 = o[3]; }
#pragma unroll
    for (int i = 0; i < 4; ++i) {
      *(__attribute__((address_space(3))) f32x4*)(mb + i * 1024) = f32x4{sa[4 * i], sa[4 * i + 1], sa[4 * i + 2], sa[4 * i + 3]};
      *(__attribute__((address_space(3))) f32x4*)(mb + 4096 + i * 1024) = f32x4{sb[4 * i], sb[4 * i + 1], sb[4 * i + 2], sb[4 * i + 3]};
    }
    *(__attribute__((address_space(3))) f32x2*)(lds + 6 * SLOT + w * 512 + lane * 8) = f32x2{m, l};
    lds_bar();
    const int w2 = w ^ 4;
    const int mslot2 = (w2 >> 1) < 2 ? 4 + (w2 >> 1) : (w2 >> 1) - 2;
    lds_cptr pb = lds + mslot2 * SLOT + (w2 & 1) * 8192 + lane * 16;
    const f32x2 ml2 = *(const __attribute__((address_space(3))) f32x2*)(lds + 6 * SLOT + w2 * 512 + lane * 8);
    const float mt = fmaxf(m, ml2[0]);
    const float a1 = __builtin_amdgcn_exp2f((m - mt) * SCL), a2 = __builtin_amdgcn_exp2f((ml2[0] - mt) * SCL);
    const float inv = 1.0f / (a1 * l + a2 * ml2[1]);
    const float f1 = a1 * inv, f2 = a2 * inv;
#pragma unroll
    for (int i = 0; i < 4; ++i) {
      const f32x4 u0 = *(const __attribute__((address_space(3))) f32x4*)(pb + i * 1024);
      const f32x4 u1 = *(const __attribute__((address_space(3))) f32x4*)(pb + 4096 + i * 1024);
#pragma unroll
      for (int j = 0; j < 4; ++j) {
        ka[4 * i + j] = ka[4 * i + j] * f1 + u0[j] * f2;
        kb2[4 * i + j] = kb2[4 * i + j] * f1 + u1[j] * f2;
      }
    }
    const int m16 = 2 * qt + (r32 >> 4);
    if (m16 < 7) {
      char* ob = ws + WS_O + ((size_t)((b * 128 + h * 4 + 2 * g) * 7 + m16)) * 1024 + (32 * hh + (r32 & 15)) * 16;
      *(bf16x8*)(ob) = pack8<0>(ka);
      *(bf16x8*)(ob + 256) = pack8<1>(ka);
      *(bf16x8*)(ob + 7 * 1024) = pack8<0>(kb2);
      *(bf16x8*)(ob + 7 * 1024 + 256) = pack8<1>(kb2);
    }
  }
}

constexpr int LDS2 = 4 * 35 * 1024 + 64;

__global__ __launch_bounds__(256, 1) void k2_oproj(char* __restrict__ ws, unsigned short* __restrict__ out) {
  extern __shared__ __attribute__((aligned(16))) char lds_raw[];
  const lds_ptr lds = (lds_ptr)lds_raw;
  const int tid = threadIdx.x, lane = tid & 63;
  const int w = __builtin_amdgcn_readfirstlane(tid >> 6);
  const int bid = blockIdx.x, xcd = bid & 7, idx = bid >> 3;
  const int kq = xcd >> 1, b = 4 * (xcd & 1) + (idx >> 3), nt = idx & 7;
  const int kb0 = 32 * kq + 8 * w;
  const char* ap = ws + WS_O + (size_t)(b * 128 + kb0) * 7 * 1024 + lane * 16;
  const char* bp = ws + WS_WOT + (size_t)(nt * 128 + kb0) * 5 * 1024 + lane * 16;

  f32x4 acc[7][5];
#pragma unroll
  for (int i = 0; i < 7; ++i)
#pragma unroll
    for (int j = 0; j < 5; ++j) acc[i][j] = f32x4{0.f, 0.f, 0.f, 0.f};
  constexpr int DEPTH = 4;
  bf16x8 af[DEPTH][7], bf[DEPTH][5];
#pragma unroll
  for (int ks = 0; ks < DEPTH; ++ks) {
#pragma unroll
    for (int j = 0; j < 5; ++j) bf[ks][j] = *(const bf16x8*)(bp + (ks * 5 + j) * 1024);
#pragma unroll
    for (int i = 0; i < 7; ++i) af[ks][i] = *(const bf16x8*)(ap + (ks * 7 + i) * 1024);
  }
#pragma unroll
  for (int ks = 0; ks < 8; ++ks) {
    const int sl = ks % DEPTH;
#pragma unroll
    for (int i = 0; i < 7; ++i)
#pragma unroll
      for (int j = 0; j < 5; ++j) acc[i][j] = MFMA16(bf[sl][j], af[sl][i], acc[i][j]);
    if (ks + DEPTH < 8) {
#pragma unroll
      for (int j = 0; j < 5; ++j) bf[sl][j] = *(const bf16x8*)(bp + ((ks + DEPTH) * 5 + j) * 1024);
#pragma unroll
      for (int i = 0; i < 7; ++i) af[sl][i] = *(const bf16x8*)(ap + ((ks + DEPTH) * 7 + i) * 1024);
    }
  }
  {
    lds_ptr rp = lds + w * 35 * 1024 + lane * 16;
#pragma unroll
    for (int i = 0; i < 7; ++i)
#pragma unroll
      for (int j = 0; j < 5; ++j) *(__attribute__((address_space(3))) f32x4*)(rp + (i * 5 + j) * 1024) = acc[i][j];
  }
  __syncthreads();
  f32x4 v[9];
  const int tile = b * 8 + nt;
  const auto prs = __builtin_amdgcn_make_buffer_rsrc((void*)(ws + WS_PART + (size_t)tile * 4 * 2240 * 16), 0,
                                                     4 * 2240 * 16, 0x00020000);
#pragma unroll
  for (int i = 0; i < 9; ++i) {
    const int e4 = tid + 256 * i;
    if (e4 < 2240) {
      lds_cptr rp = lds + e4 * 16;
      f32x4 t = *(const __attribute__((address_space(3))) f32x4*)(rp);
      t += *(const __attribute__((address_space(3))) f32x4*)(rp + 35 * 1024);
      t += *(const __attribute__((address_space(3))) f32x4*)(rp + 70 * 1024);
      t += *(const __attribute__((address_space(3))) f32x4*)(rp + 105 * 1024);
      v[i] = t;
      __builtin_amdgcn_raw_buffer_store_b128(__builtin_bit_cast(u32x4, t), prs, (kq * 2240 + e4) * 16, 0, 16);
    } else {
      v[i] = f32x4{0.f, 0.f, 0.f, 0.f};
    }
  }
  asm volatile("s_waitcnt vmcnt(0)" ::: "memory");
  __syncthreads();
  if (tid == 0) {
    const unsigned old = __hip_atomic_fetch_add((unsigned*)(ws + WS_CNT + tile * 64), 1u, __ATOMIC_RELAXED,
                                                __HIP_MEMORY_SCOPE_AGENT);
    *(__attribute__((address_space(3))) unsigned*)(lds + 4 * 35 * 1024) = old;
  }
  __syncthreads();
  const unsigned old = *(const __attribute__((address_space(3))) unsigned*)(lds + 4 * 35 * 1024);
  if (old != 3u) return;
  __builtin_amdgcn_fence(__ATOMIC_ACQUIRE, "wavefront");
#pragma unroll
  for (int i = 0; i < 9; ++i) {
    const int e4 = tid + 256 * i;
    if (e4 < 2240) {
      f32x4 t = {0.f, 0.f, 0.f, 0.f};
#pragma unroll
      for (int q = 0; q < 4; ++q) {
        const u32x4 u = __builtin_amdgcn_raw_buffer_load_b128(prs, (q * 2240 + e4) * 16, 0, 16);
        const f32x4 pv = __builtin_bit_cast(f32x4, u);
        t += (q == kq) ? v[i] : pv;
      }
      const int ti = e4 >> 6, ln = e4 & 63, mi = ti / 5, ni = ti % 5;
      const int m = 16 * mi + (ln & 15), n = 80 * nt + 16 * ni + 4 * (ln >> 4);
      u32x2 o2 = {cvtpk(t[0], t[1]), cvtpk(t[2], t[3])};
      *(u32x2*)(out + (size_t)(b * SQ + m) * DM + n) = o2;
    }
  }
}

}

extern "C" void kernel_launch(void* const* d_in, const int* in_sizes, int n_in, void* d_out, int out_size, void* d_ws,
                              size_t ws_size, hipStream_t stream) {
  (void)in_sizes; (void)n_in; (void)out_size; (void)ws_size;
  (void)hipFuncSetAttribute((const void*)k1_attn, hipFuncAttributeMaxDynamicSharedMemorySize, LDS1);
  (void)hipFuncSetAttribute((const void*)k2_oproj, hipFuncAttributeMaxDynamicSharedMemorySize, LDS2);
  hipLaunchKernelGGL(k1_attn, dim3(256), dim3(512), LDS1, stream, (const char*)d_in[0], (const char*)d_in[1],
                     (const char*)d_in[2], (const char*)d_in[3], (const unsigned short*)d_in[4], (char*)d_ws);
  hipLaunchKernelGGL(k2_oproj, dim3(256), dim3(256), LDS2, stream, (char*)d_ws, (unsigned short*)d_out);
}
```

```cpp
#include <hip/hip_runtime.h>
#include <hip/hip_bf16.h>
#include <stdint.h>

namespace {

using bf16x8 = __attribute__((ext_vector_type(8))) short;
using s16x4 = __attribute__((ext_vector_type(4))) short;
using f32x16 = __attribute__((ext_vector_type(16))) float;
using f32x4 = __attribute__((ext_vector_type(4))) float;
using f32x2 = __attribute__((ext_vector_type(2))) float;
using u32x4 = __attribute__((ext_vector_type(4))) unsigned;
using u32x2 = __attribute__((ext_vector_type(2))) unsigned;
typedef short v4i16_t __attribute__((ext_vector_type(4)));
typedef __attribute__((address_space(3))) char* lds_ptr;
typedef __attribute__((address_space(3))) const char* lds_cptr;

#define DI __device__ __forceinline__
#define SBAR() __builtin_amdgcn_sched_barrier(0)
#define MFMA32(a, b, c) __builtin_amdgcn_mfma_f32_32x32x16_bf16((a), (b), (c), 0, 0, 0)
#define MFMA16(a, b, c) __builtin_amdgcn_mfma_f32_16x16x32_bf16((a), (b), (c), 0, 0, 0)

constexpr int NB = 8, SQ = 112, DM = 640, NH = 32, SK = 512, DH = 128;
constexpr int SLOT = 16384;
constexpr int LDS1 = 10 * SLOT;
constexpr float SCL = 0.08838834764831845f * 1.4426950408889634f;

constexpr size_t WS_CNT = 0;
constexpr size_t WS_O = 4096;
constexpr size_t WS_O_BYTES = (size_t)NB * 128 * 7 * 1024;
constexpr size_t WS_WOT = WS_O + WS_O_BYTES;
constexpr size_t WS_WOT_BYTES = (size_t)8 * 128 * 5 * 1024;
constexpr size_t WS_PART = WS_WOT + WS_WOT_BYTES;
constexpr size_t WS_PART_BYTES = (size_t)64 * 4 * 2240 * 16;

#define STAMP(i) do {} while (0)
#define STAMP2(i) do {} while (0)
DI s16x4 vtr(lds_cptr p) {
  return __builtin_bit_cast(s16x4, __builtin_amdgcn_ds_read_tr16_b64_v4i16((__attribute__((address_space(3))) v4i16_t*)p));
}
DI bf16x8 ldsr128(lds_cptr p) { return *(const __attribute__((address_space(3))) bf16x8*)p; }
DI bf16x8 cat8(s16x4 lo, s16x4 hi) { return __builtin_shufflevector(lo, hi, 0, 1, 2, 3, 4, 5, 6, 7); }
typedef __bf16 bfv2 __attribute__((ext_vector_type(2)));
DI unsigned cvtpk(float lo, float hi) {
  const f32x2 v = {lo, hi};
  return __builtin_bit_cast(unsigned, __builtin_convertvector(v, bfv2));
}
template <int S>
DI bf16x8 pack8(const f32x16& x) {
  u32x4 p = {cvtpk(x[8 * S + 0], x[8 * S + 1]), cvtpk(x[8 * S + 2], x[8 * S + 3]), cvtpk(x[8 * S + 4], x[8 * S + 5]),
             cvtpk(x[8 * S + 6], x[8 * S + 7])};
  return __builtin_bit_cast(bf16x8, p);
}
template <bool NT>
DI void glds16(const void* sbase, unsigned voff, unsigned lds_dst) {
  unsigned sv;
  if constexpr (NT)
    asm volatile("s_mov_b32 %0, m0\n\ts_mov_b32 m0, %3\n\ts_nop 2\n\tglobal_load_lds_dwordx4 %1, %2 nt\n\ts_mov_b32 m0, %0"
                 : "=&s"(sv) : "v"(voff), "s"(sbase), "s"(lds_dst) : "memory");
  else
    asm volatile("s_mov_b32 %0, m0\n\ts_mov_b32 m0, %3\n\ts_nop 2\n\tglobal_load_lds_dwordx4 %1, %2\n\ts_mov_b32 m0, %0"
                 : "=&s"(sv) : "v"(voff), "s"(sbase), "s"(lds_dst) : "memory");
}
template <int N>
DI void wait_bar() {
  asm volatile("s_waitcnt vmcnt(%0) lgkmcnt(0)\n\ts_barrier" ::"i"(N) : "memory");
}
template <bool WT>
DI void store16(char* p, bf16x8 v) {
  if constexpr (WT) asm volatile("global_store_dwordx4 %0, %1, off sc1\n\ts_nop 1" ::"v"(p), "v"(v) : "memory");
  else *(bf16x8*)p = v;
}
#define WT_O false
#define WT_W true
DI void lds_bar() { asm volatile("s_waitcnt lgkmcnt(0)\n\ts_barrier" ::: "memory"); }

constexpr int sslot(int i) { return (2 * i) % 10; }
constexpr int kvslot(int it) { return it % 6; }
constexpr int XSLOT = 6, WOSLOT = 8;

struct K1Ctx {
  const char* xb;
  const char* wqb;
  const char* kb;
  const char* vb;
  unsigned vx0, vx1, vw0, vw1, vk0, vk1, vv0, vv1;
  unsigned ldsw;
  int rot;
};

#define NTP false
#define ROTN 10
template <int I, int J>
DI void dma_s(const K1Ctx& c) {
  constexpr int sl = sslot(I);
  int st = I + c.rot;
  st = st >= 10 ? st - 10 : st;
  if constexpr (J == 0) glds16<NTP>(c.xb + st * 128, c.vx0, c.ldsw + sl * SLOT);
  if constexpr (J == 1) glds16<NTP>(c.wqb + (size_t)st * 64 * 8192, c.vw0, c.ldsw + (sl + 1) * SLOT);
  if constexpr (J == 2) glds16<NTP>(c.xb + st * 128, c.vx1, c.ldsw + sl * SLOT + 1024);
  if constexpr (J == 3) glds16<NTP>(c.wqb + (size_t)st * 64 * 8192, c.vw1, c.ldsw + (sl + 1) * SLOT + 1024);
}
constexpr bool pos_is_k(int n) { return n == 0 || ((n & 1) && n < 15); }
constexpr int pos_tile(int n) { return n == 0 ? 0 : (n == 15 ? 7 : ((n & 1) ? (n + 1) / 2 : n / 2 - 1)); }
template <int IT, int J>
DI void dma_kv(const K1Ctx& c) {
  if constexpr (pos_is_k(IT))
    glds16<true>(c.kb + pos_tile(IT) * 16384, J ? c.vk1 : c.vk0, c.ldsw + kvslot(IT) * SLOT + J * 1024);
  else
    glds16<true>(c.vb + pos_tile(IT) * 16384, J ? c.vv1 : c.vv0, c.ldsw + kvslot(IT) * SLOT + J * 1024);
}
template <int IT>
DI void dma_kv2(const K1Ctx& c) { dma_kv<IT, 0>(c); dma_kv<IT, 1>(c); }
#define RA 3
template <int I, int J>
DI void dma_in_stage(const K1Ctx& c) {
  if constexpr (I <= 6) dma_s<I + 3, J>(c);
  else if constexpr ((I - 7) * 2 + J / 2 < RA) dma_kv<(I - 7) * 2 + J / 2, J % 2>(c);
}
template <int N>
DI void wait_vm() { asm volatile("s_waitcnt vmcnt(%0)" ::"i"(N) : "memory"); }

struct PFrag { bf16x8 x, a0, a1; };
template <int I, int KS>
DI PFrag proj_load(lds_cptr lds, unsigned xoff, unsigned woff0, unsigned woff1) {
  lds_cptr xs = lds + sslot(I) * SLOT, ws = lds + (sslot(I) + 1) * SLOT;
  PFrag f;
  f.x = ldsr128(xs + (xoff ^ (unsigned)(KS * 32)));
  f.a0 = cat8(vtr(ws + woff0 + KS * 4096), vtr(ws + woff0 + KS * 4096 + 1024));
  f.a1 = cat8(vtr(ws + woff1 + KS * 4096), vtr(ws + woff1 + KS * 4096 + 1024));
  return f;
}
#define PMFMA(a, b, c) MFMA32(a, b, c)
template <int I>
DI void proj_stage(const K1Ctx& c, lds_cptr lds, unsigned xoff, unsigned woff0, unsigned woff1, f32x16& q0, f32x16& q1,
                   PFrag& f0, PFrag& f1) {
  PFrag f2 = proj_load<I, 2>(lds, xoff, woff0, woff1);
  SBAR();
  q0 = PMFMA(f0.a0, f0.x, q0);
  q1 = PMFMA(f0.a1, f0.x, q1);
  SBAR();
  dma_in_stage<I, 0>(c);
  PFrag f3 = proj_load<I, 3>(lds, xoff, woff0, woff1);
  SBAR();
  q0 = PMFMA(f1.a0, f1.x, q0);
  q1 = PMFMA(f1.a1, f1.x, q1);
  SBAR();
  dma_in_stage<I, 1>(c);
  if constexpr (I < 9) f0 = proj_load<I + 1, 0>(lds, xoff, woff0, woff1);
  SBAR();
  q0 = PMFMA(f2.a0, f2.x, q0);
  q1 = PMFMA(f2.a1, f2.x, q1);
  SBAR();
  dma_in_stage<I, 2>(c);
  if constexpr (I < 9) f1 = proj_load<I + 1, 1>(lds, xoff, woff0, woff1);
  SBAR();
  q0 = PMFMA(f3.a0, f3.x, q0);
  q1 = PMFMA(f3.a1, f3.x, q1);
  SBAR();
  dma_in_stage<I, 3>(c);
  if constexpr (I <= 7) wait_bar<4>();
  else if constexpr (I == 8) lds_bar();
}

DI float max16(const f32x16& s) {
  float a = fmaxf(fmaxf(s[0], s[1]), s[2]), b = fmaxf(fmaxf(s[3], s[4]), s[5]);
  a = fmaxf(fmaxf(a, s[6]), s[7]);
  b = fmaxf(fmaxf(b, s[8]), s[9]);
  a = fmaxf(fmaxf(a, s[10]), s[11]);
  b = fmaxf(fmaxf(b, s[12]), s[13]);
  a = fmaxf(fmaxf(a, s[14]), s[15]);
  return fmaxf(a, b);
}
DI float swap32(float v) {
  auto r = __builtin_amdgcn_permlane32_swap(__float_as_uint(v), __float_as_uint(v), false, false);
  return (threadIdx.x & 32) ? __uint_as_float(r[0]) : __uint_as_float(r[1]);
}
DI float max_halves(float v) {
  auto r = __builtin_amdgcn_permlane32_swap(__float_as_uint(v), __float_as_uint(v), false, false);
  return fmaxf(__uint_as_float(r[0]), __uint_as_float(r[1]));
}
DI float sum_halves(float v) {
  auto r = __builtin_amdgcn_permlane32_swap(__float_as_uint(v), __float_as_uint(v), false, false);
  return __uint_as_float(r[0]) + __uint_as_float(r[1]);
}
constexpr float THR_LOG2 = 16.f;
constexpr float THR_RAW = THR_LOG2 / SCL;
struct SmState { float mref, l, fs, nmc; bool moved; };

template <int N>
DI void step_sync(const K1Ctx& c) {
  wait_bar<(N + RA - 1 <= 15 ? 2 * (RA - 1) : 2 * (15 - N))>();
  if constexpr (N + RA <= 15) dma_kv2<N + RA>(c);
}
template <bool FIRST>
DI void sm_decide(const f32x16& s, SmState& st) {
  const float tm = max_halves(max16(s));
  if constexpr (FIRST) {
    st.mref = tm;
    st.fs = 1.f;
    st.moved = false;
  } else {
    st.fs = 1.f;
    st.moved = false;
    if (__builtin_expect(__any(tm - st.mref > THR_RAW), 0)) {
      const float mn = fmaxf(st.mref, tm);
      st.fs = __builtin_amdgcn_exp2f((st.mref - mn) * SCL);
      st.mref = mn;
      st.moved = true;
    }
  }
  st.nmc = -st.mref * SCL;
}
DI unsigned sm_pair(const f32x16& s, int i, float nmc, float& la, float& lb) {
  const float e0 = __builtin_amdgcn_exp2f(__builtin_fmaf(s[2 * i], SCL, nmc));
  const float e1 = __builtin_amdgcn_exp2f(__builtin_fmaf(s[2 * i + 1], SCL, nmc));
  la += e0;
  lb += e1;
  asm volatile("" : "+v"(la), "+v"(lb));
  return cvtpk(e0, e1);
}
template <int N, bool SM>
DI void k_step(const K1Ctx& c, lds_cptr lds, unsigned koff, const bf16x8 (&qf)[8], f32x16& sn, const f32x16& so,
               SmState& st, u32x4& p0, u32x4& p1) {
  step_sync<N>(c);
  unsigned kb = koff + kvslot(N) * SLOT;
  asm volatile("" : "+v"(kb));
  bf16x8 kf[8];
#pragma unroll
  for (int ks = 0; ks < 8; ++ks) kf[ks] = ldsr128(lds + (kb ^ (unsigned)(ks * 32)));
  float la = 0.f, lb = 0.f;
  const float nmc = st.nmc;
  sn = f32x16{};
  SBAR();
#pragma unroll
  for (int ks = 0; ks < 8; ++ks) {
    sn = MFMA32(kf[ks], qf[ks], sn);
    if constexpr (SM) {
      const unsigned wd = sm_pair(so, ks, nmc, la, lb);
      if (ks < 4) p0[ks] = wd; else p1[ks - 4] = wd;
    }
    SBAR();
  }
  if constexpr (SM) st.l = st.l * st.fs + (la + lb);
}
template <int N, bool SM>
DI void v_step(const K1Ctx& c, lds_cptr lds, const unsigned (&voff)[4], const u32x4& p0, const u32x4& p1, float fs,
               bool moved, f32x16 (&o)[4], const f32x16& so, SmState& st, u32x4& q0, u32x4& q1) {
  step_sync<N>(c);
  unsigned vb = voff[0] + kvslot(N) * SLOT;
  asm volatile("" : "+v"(vb));
  bf16x8 vf[8];
#pragma unroll
  for (int dt = 0; dt < 4; ++dt) {
    lds_cptr vp = lds + (vb ^ (unsigned)(dt * 64));
    vf[2 * dt] = cat8(vtr(vp), vtr(vp + 2048));
    vf[2 * dt + 1] = cat8(vtr(vp + 4096), vtr(vp + 4096 + 2048));
  }
  if (__builtin_expect(moved, 0)) {
#pragma unroll
    for (int dt = 0; dt < 4; ++dt) o[dt] *= fs;
  }
  float la = 0.f, lb = 0.f;
  const float nmc = st.nmc;
  SBAR();
#pragma unroll
  for (int i = 0; i < 8; ++i) {
    o[i >> 1] = MFMA32(vf[i], __builtin_bit_cast(bf16x8, (i & 1) ? p1 : p0), o[i >> 1]);
    if constexpr (SM) {
      const unsigned wd = sm_pair(so, i, nmc, la, lb);
      if (i < 4) q0[i] = wd; else q1[i - 4] = wd;
    }
    SBAR();
  }
  if constexpr (SM) st.l = st.l * st.fs + (la + lb);
}

__global__ __launch_bounds__(512, 2) void k1_attn(const char* __restrict__ x, const char* __restrict__ kc,
                                                   const char* __restrict__ vc, const char* __restrict__ wq,
                                                   const unsigned short* __restrict__ wo, char* __restrict__ ws) {
  extern __shared__ __attribute__((aligned(16))) char lds_raw[];
  const lds_ptr lds = (lds_ptr)lds_raw;
  const int tid = threadIdx.x, lane = tid & 63;
  const int w = __builtin_amdgcn_readfirstlane(tid >> 6);
  const int qt = w & 3, g = w >> 2;
  const int bid = blockIdx.x, xcd = bid & 7, idx = bid >> 3;
#define HPX 8
  const int h = (xcd % (32 / HPX)) * HPX + (idx % HPX), b = (xcd / (32 / HPX)) * (32 / HPX) + idx / HPX;
  const int r32 = lane & 31, hh = lane >> 5, blk = (lane >> 4) & 1, q4 = (lane & 15) >> 2, p = lane & 3;

  if (bid == 0 && tid < 64) *(unsigned*)(ws + WS_CNT + tid * 64) = 0u;

  STAMP(0);
  K1Ctx c;
  c.xb = x + (size_t)b * SQ * DM * 2;
  c.wqb = wq + h * 256;
  c.kb = kc + (size_t)(b * NH + h) * SK * DH * 2;
  c.vb = vc + (size_t)(b * NH + h) * SK * DH * 2;
  {
    const int xr0 = 16 * w + (lane >> 3), xr1 = xr0 + 8;
    c.vx0 = xr0 < SQ ? (unsigned)(xr0 * 1280 + (((lane & 7) ^ ((xr0 >> 1) & 7)) << 4)) : 0u;
    c.vx1 = xr1 < SQ ? (unsigned)(xr1 * 1280 + (((lane & 7) ^ ((xr1 >> 1) & 7)) << 4)) : 0u;
    const int r0 = 8 * w + (lane >> 4), r1 = r0 + 4;
    c.vw0 = (unsigned)(r0 * 8192 + (((lane & 15) ^ ((r0 & 3) << 2)) << 4));
    c.vw1 = (unsigned)(r1 * 8192 + (((lane & 15) ^ ((r1 & 3) << 2)) << 4));
    c.vk0 = (unsigned)(r0 * 256 + (((lane & 15) ^ (r0 & 15)) << 4));
    c.vk1 = (unsigned)(r1 * 256 + (((lane & 15) ^ (r1 & 15)) << 4));
    c.vv0 = (unsigned)(r0 * 256 + (((lane & 15) ^ ((r0 & 3) << 2)) << 4));
    c.vv1 = (unsigned)(r1 * 256 + (((lane & 15) ^ ((r1 & 3) << 2)) << 4));
  }
  c.ldsw = (unsigned)(uintptr_t)lds_raw + (unsigned)w * 2048u;
  c.rot = ROTN == 10 ? (((b & 3) * 5) / 2 + ((h & 7) * 5) / 4) % 10 : (((b + (h & 7)) % ROTN) * 10) / ROTN;

  dma_s<0, 0>(c); dma_s<0, 1>(c); dma_s<0, 2>(c); dma_s<0, 3>(c);
  dma_s<1, 0>(c); dma_s<1, 1>(c); dma_s<1, 2>(c); dma_s<1, 3>(c);
  dma_s<2, 0>(c); dma_s<2, 1>(c); dma_s<2, 2>(c); dma_s<2, 3>(c);

  f32x16 q0 = {}, q1 = {};
  {
    const int xrow = 32 * qt + r32;
    const unsigned xoff = (unsigned)(xrow * 128 + ((hh ^ ((xrow >> 1) & 7)) << 4));
    const int sp = ((p & 1) << 1) | (p >> 1);
    const int d0 = 2 * g, d1 = 2 * g + 1;
    const unsigned wrow = (unsigned)((8 * hh + q4) * 256 + 8 * (sp & 1));
    const unsigned woff0 = wrow + (unsigned)((4 * (d0 ^ q4) + 2 * blk + (sp >> 1)) << 4);
    const unsigned woff1 = wrow + (unsigned)((4 * (d1 ^ q4) + 2 * blk + (sp >> 1)) << 4);
    wait_bar<4>();
    PFrag f0 = proj_load<0, 0>(lds, xoff, woff0, woff1);
    PFrag f1 = proj_load<0, 1>(lds, xoff, woff0, woff1);
    proj_stage<0>(c, lds, xoff, woff0, woff1, q0, q1, f0, f1);
    STAMP(1);
    proj_stage<1>(c, lds, xoff, woff0, woff1, q0, q1, f0, f1);
    STAMP(2);
    proj_stage<2>(c, lds, xoff, woff0, woff1, q0, q1, f0, f1);
    STAMP(3);
    proj_stage<3>(c, lds, xoff, woff0, woff1, q0, q1, f0, f1);
    STAMP(4);
    proj_stage<4>(c, lds, xoff, woff0, woff1, q0, q1, f0, f1);
    STAMP(5);
    proj_stage<5>(c, lds, xoff, woff0, woff1, q0, q1, f0, f1);
    STAMP(6);
    proj_stage<6>(c, lds, xoff, woff0, woff1, q0, q1, f0, f1);
    STAMP(7);
    proj_stage<7>(c, lds, xoff, woff0, woff1, q0, q1, f0, f1);
    STAMP(8);
    proj_stage<8>(c, lds, xoff, woff0, woff1, q0, q1, f0, f1);
    STAMP(9);
    proj_stage<9>(c, lds, xoff, woff0, woff1, q0, q1, f0, f1);
    STAMP(10);
  }
  bf16x8 qf[8];
  {
    lds_ptr e = lds + XSLOT * SLOT + qt * 8192 + g * 4096 + lane * 16;
    *(__attribute__((address_space(3))) bf16x8*)(e) = pack8<0>(q0);
    *(__attribute__((address_space(3))) bf16x8*)(e + 1024) = pack8<1>(q0);
    *(__attribute__((address_space(3))) bf16x8*)(e + 2048) = pack8<0>(q1);
    *(__attribute__((address_space(3))) bf16x8*)(e + 3072) = pack8<1>(q1);
  }
  lds_bar();
  {
    lds_cptr e = lds + XSLOT * SLOT + qt * 8192 + lane * 16;
#pragma unroll
    for (int ks = 0; ks < 8; ++ks) qf[ks] = ldsr128(e + ks * 1024);
  }
  STAMP(11);
  f32x16 o[4] = {};
  SmState st;
  st.l = 0.f;
  {
    const int krow = 32 * g + r32;
    const unsigned koff = (unsigned)(krow * 256 + ((hh ^ (krow & 15)) << 4));
    unsigned voff[4];
    const unsigned vrow = (unsigned)((32 * g + 4 * hh + q4) * 256 + 8 * (p & 1));
#pragma unroll
    for (int dt = 0; dt < 4; ++dt) voff[dt] = vrow + (unsigned)((4 * (dt ^ q4) + 2 * blk + (p >> 1)) << 4);
    f32x16 sA, sB;
    u32x4 pa0, pa1, pb0, pb1;
    float fsp; bool mvp;
#define KSTEP(N, SN, SO, P0, P1)                                   \
    fsp = st.fs; mvp = st.moved;                                   \
    k_step<N, true>(c, lds, koff, qf, SN, SO, st, P0, P1);         \
    sm_decide<false>(SN, st);
#define VSTEP(N, P0, P1) v_step<N, false>(c, lds, voff, P0, P1, fsp, mvp, o, sA, st, pb0, pb1);
    k_step<0, false>(c, lds, koff, qf, sA, sA, st, pa0, pa1);
    sm_decide<true>(sA, st);
    KSTEP(1, sB, sA, pa0, pa1)   VSTEP(2, pa0, pa1)
    KSTEP(3, sA, sB, pa0, pa1)   VSTEP(4, pa0, pa1)
    KSTEP(5, sB, sA, pa0, pa1)   VSTEP(6, pa0, pa1)
    if (w < 5) {
      const char* src = (const char*)wo + (size_t)(bid >> 1) * 32 * 1280 + ((bid & 1) * 5 + w) * 128;
      const unsigned vo = (unsigned)((lane >> 3) * 1280 + (lane & 7) * 16);
      const unsigned dst = (unsigned)(uintptr_t)lds_raw + WOSLOT * SLOT + (unsigned)w * 4096u;
      glds16<true>(src, vo, dst);
      glds16<true>(src + 8 * 1280, vo, dst + 1024);
      glds16<true>(src + 16 * 1280, vo, dst + 2048);
      glds16<true>(src + 24 * 1280, vo, dst + 3072);
    }
    KSTEP(7, sA, sB, pa0, pa1)   VSTEP(8, pa0, pa1)
    KSTEP(9, sB, sA, pa0, pa1)   VSTEP(10, pa0, pa1)
    if (w < 5) {
      const int gg = lane >> 4;
      lds_cptr wp = lds + WOSLOT * SLOT + w * 4096 + (16 * (gg & 1) + 4 * (gg >> 1) + q4) * 128 + p * 8;
#pragma unroll
      for (int i = 0; i < 4; ++i) {
        const bf16x8 f = cat8(vtr(wp + i * 32), vtr(wp + i * 32 + 8 * 128));
        const int nb = (bid & 1) * 20 + w * 4 + i;
        store16<WT_W>(ws + WS_WOT + ((size_t)((nb / 5) * 128 + (bid >> 1)) * 5 + (nb % 5)) * 1024 + lane * 16, f);
      }
    }
    KSTEP(11, sA, sB, pa0, pa1)  VSTEP(12, pa0, pa1)
    KSTEP(13, sB, sA, pa0, pa1)
    STAMP(12);
    {
      const float fs7 = st.fs; const bool mv7 = st.moved;
      v_step<14, true>(c, lds, voff, pa0, pa1, fsp, mvp, o, sB, st, pb0, pb1);
      v_step<15, false>(c, lds, voff, pb0, pb1, fs7, mv7, o, sA, st, pa0, pa1);
    }
    STAMP(22);
#undef KSTEP
#undef VSTEP
  }
  const float m = st.mref;
  const float l = sum_halves(st.l);
  {
    const int mslot = 6 + (w >> 1);
    lds_ptr mb = lds + mslot * SLOT + (w & 1) * 8192 + lane * 16;
    f32x16 sa, sb, ka, kb2;
    if (g == 0) { sa = o[2]; sb = o[3]; ka = o[0]; kb2 = o[1]; }
    else        { sa = o[0]; sb = o[1]; ka = o[2]; kb2 = o[3]; }
#pragma unroll
    for (int i = 0; i < 4; ++i) {
      *(__attribute__((address_space(3))) f32x4*)(mb + i * 1024) = f32x4{sa[4 * i], sa[4 * i + 1], sa[4 * i + 2], sa[4 * i + 3]};
      *(__attribute__((address_space(3))) f32x4*)(mb + 4096 + i * 1024) = f32x4{sb[4 * i], sb[4 * i + 1], sb[4 * i + 2], sb[4 * i + 3]};
    }
    *(__attribute__((address_space(3))) f32x2*)(lds + w * 512 + lane * 8) = f32x2{m, l};
    lds_bar();
    const int w2 = w ^ 4;
    const int mslot2 = 6 + (w2 >> 1);
    lds_cptr pb = lds + mslot2 * SLOT + (w2 & 1) * 8192 + lane * 16;
    const f32x2 ml2 = *(const __attribute__((address_space(3))) f32x2*)(lds + w2 * 512 + lane * 8);
    const float mt = fmaxf(m, ml2[0]);
    const float a1 = __builtin_amdgcn_exp2f((m - mt) * SCL), a2 = __builtin_amdgcn_exp2f((ml2[0] - mt) * SCL);
    const float inv = 1.0f / (a1 * l + a2 * ml2[1]);
    const float f1 = a1 * inv, f2 = a2 * inv;
#pragma unroll
    for (int i = 0; i < 4; ++i) {
      const f32x4 u0 = *(const __attribute__((address_space(3))) f32x4*)(pb + i * 1024);
      const f32x4 u1 = *(const __attribute__((address_space(3))) f32x4*)(pb + 4096 + i * 1024);
#pragma unroll
      for (int j = 0; j < 4; ++j) {
        ka[4 * i + j] = ka[4 * i + j] * f1 + u0[j] * f2;
        kb2[4 * i + j] = kb2[4 * i + j] * f1 + u1[j] * f2;
      }
    }
    const int m16 = 2 * qt + (r32 >> 4);
    if (m16 < 7) {
      char* ob = ws + WS_O + ((size_t)((b * 128 + h * 4 + 2 * g) * 7 + m16)) * 1024 + (32 * hh + (r32 & 15)) * 16;
      store16<WT_O>(ob, pack8<0>(ka));
      store16<WT_O>(ob + 256, pack8<1>(ka));
      store16<WT_O>(ob + 7 * 1024, pack8<0>(kb2));
      store16<WT_O>(ob + 7 * 1024 + 256, pack8<1>(kb2));
    }
    STAMP(23);
  }
}

constexpr int LDS2 = 4 * 35 * 1024 + 64;

__global__ __launch_bounds__(256, 1) void k2_oproj(char* __restrict__ ws, unsigned short* __restrict__ out) {
  extern __shared__ __attribute__((aligned(16))) char lds_raw[];
  const lds_ptr lds = (lds_ptr)lds_raw;
  const int tid = threadIdx.x, lane = tid & 63;
  const int w = __builtin_amdgcn_readfirstlane(tid >> 6);
  const int bid = blockIdx.x, xcd = bid & 7, idx = bid >> 3;
  const int kq = xcd & 3, b = 4 * (xcd >> 2) + (idx >> 3), nt = idx & 7;
  const int kb0 = 32 * kq + 8 * w;
  const char* ap = ws + WS_O + (size_t)(b * 128 + kb0) * 7 * 1024 + lane * 16;
  const char* bp = ws + WS_WOT + (size_t)(nt * 128 + kb0) * 5 * 1024 + lane * 16;
  const int rot = nt + 2 * (b & 3);

  STAMP2(0);
  f32x4 acc[7][5];
#pragma unroll
  for (int i = 0; i < 7; ++i)
#pragma unroll
    for (int j = 0; j < 5; ++j) acc[i][j] = f32x4{0.f, 0.f, 0.f, 0.f};
#define K2DEPTH 3
  constexpr int DEPTH = K2DEPTH;
  bf16x8 fr[DEPTH][12];
  auto ldf = [&](int kr, int f) {
    return f < 5 ? *(const bf16x8*)(bp + (kr * 5 + f) * 1024) : *(const bf16x8*)(ap + (kr * 7 + (f - 5)) * 1024);
  };
#pragma unroll
  for (int ks = 0; ks < DEPTH - 1; ++ks) {
    const int kr = (ks + rot) & 7;
#pragma unroll
    for (int f = 0; f < 12; ++f) fr[ks][f] = ldf(kr, f);
    SBAR();
  }
#pragma unroll
  for (int ks = 0; ks < 8; ++ks) {
    const int sl = ks % DEPTH, nl = (ks + DEPTH - 1) % DEPTH;
    const int kr = (ks + DEPTH - 1 + rot) & 7;
#pragma unroll
    for (int n = 0; n < 35; ++n) {
      const int i = n / 5, j = n % 5;
      acc[i][j] = MFMA16(fr[sl][j], fr[sl][5 + i], acc[i][j]);
      if (n % 3 == 0 && n / 3 < 12 && ks + DEPTH - 1 < 8) {
        fr[nl][n / 3] = ldf(kr, n / 3);
        SBAR();
      }
    }
    SBAR();
  }
  STAMP2(1);
  {
    lds_ptr rp = lds + w * 35 * 1024 + lane * 16;
#pragma unroll
    for (int i = 0; i < 7; ++i)
#pragma unroll
      for (int j = 0; j < 5; ++j) *(__attribute__((address_space(3))) f32x4*)(rp + (i * 5 + j) * 1024) = acc[i][j];
  }
  __syncthreads();
  STAMP2(2);
  const int tile = b * 8 + nt;
  const auto prs = __builtin_amdgcn_make_buffer_rsrc((void*)(ws + WS_PART + (size_t)tile * 4 * 2240 * 16), 0,
                                                     4 * 2240 * 16, 0x00020000);
  auto lsum = [&](int e4) {
    lds_cptr rp = lds + e4 * 16;
    f32x4 t = *(const __attribute__((address_space(3))) f32x4*)(rp);
    t += *(const __attribute__((address_space(3))) f32x4*)(rp + 35 * 1024);
    t += *(const __attribute__((address_space(3))) f32x4*)(rp + 70 * 1024);
    t += *(const __attribute__((address_space(3))) f32x4*)(rp + 105 * 1024);
    return t;
  };
#pragma unroll
  for (int i = 0; i < 7; ++i) {
    const int idx2 = tid + 256 * i;
    if (idx2 < 1680) {
      const int e4 = idx2 < 560 * kq ? idx2 : idx2 + 560;
      __builtin_amdgcn_raw_buffer_store_b128(__builtin_bit_cast(u32x4, lsum(e4)), prs, (kq * 2240 + e4) * 16, 0, 16);
    }
  }
  f32x4 v[3];
#pragma unroll
  for (int i = 0; i < 3; ++i) {
    const int j = tid + 256 * i;
    v[i] = lsum(560 * kq + (j < 560 ? j : 0));
  }
  asm volatile("s_waitcnt vmcnt(0)" ::: "memory");
  __syncthreads();
  STAMP2(3);
  if (tid == 0) {
    unsigned* cnt = (unsigned*)(ws + WS_CNT + tile * 64);
    __hip_atomic_fetch_add(cnt, 1u, __ATOMIC_RELAXED, __HIP_MEMORY_SCOPE_AGENT);
    for (int spin = 0; spin < (1 << 24); ++spin) {
      if (__hip_atomic_load(cnt, __ATOMIC_RELAXED, __HIP_MEMORY_SCOPE_AGENT) >= 4u) break;
      __builtin_amdgcn_s_sleep(2);
    }
  }
  __syncthreads();
  STAMP2(4);
  __builtin_amdgcn_fence(__ATOMIC_ACQUIRE, "wavefront");
  u32x4 u[3][3];
#pragma unroll
  for (int i = 0; i < 3; ++i) {
    const int j = tid + 256 * i;
    const int e4 = 560 * kq + (j < 560 ? j : 0);
#pragma unroll
    for (int q = 0; q < 3; ++q) {
      const int qq = q < kq ? q : q + 1;
      u[i][q] = __builtin_amdgcn_raw_buffer_load_b128(prs, (qq * 2240 + e4) * 16, 0, 16);
    }
  }
#pragma unroll
  for (int i = 0; i < 3; ++i) {
    const int j = tid + 256 * i;
    const f32x4 p0 = __builtin_bit_cast(f32x4, u[i][0]), p1 = __builtin_bit_cast(f32x4, u[i][1]),
                p2 = __builtin_bit_cast(f32x4, u[i][2]);
    const f32x4 a0 = kq == 0 ? v[i] : p0;
    const f32x4 a1 = kq == 1 ? v[i] : (kq < 1 ? p0 : p1);
    const f32x4 a2 = kq == 2 ? v[i] : (kq < 2 ? p1 : p2);
    const f32x4 a3 = kq == 3 ? v[i] : p2;
    const f32x4 t = ((a0 + a1) + a2) + a3;
    if (j < 560) {
      const int e4 = 560 * kq + j;
      const int ti = e4 >> 6, ln = e4 & 63, mi = ti / 5, ni = ti % 5;
      const int m = 16 * mi + (ln & 15), n = 80 * nt + 16 * ni + 4 * (ln >> 4);
      u32x2 o2 = {cvtpk(t[0], t[1]), cvtpk(t[2], t[3])};
      *(u32x2*)(out + (size_t)(b * SQ + m) * DM + n) = o2;
    }
  }
}

}

extern "C" void kernel_launch(void* const* d_in, const int* in_sizes, int n_in, void* d_out, int out_size, void* d_ws,
                              size_t ws_size, hipStream_t stream) {
  (void)in_sizes; (void)n_in; (void)out_size; (void)ws_size;
  (void)hipFuncSetAttribute((const void*)k1_attn, hipFuncAttributeMaxDynamicSharedMemorySize, LDS1);
  (void)hipFuncSetAttribute((const void*)k2_oproj, hipFuncAttributeMaxDynamicSharedMemorySize, LDS2);
  hipLaunchKernelGGL(k1_attn, dim3(256), dim3(512), LDS1, stream, (const char*)d_in[0], (const char*)d_in[1],
                     (const char*)d_in[2], (const char*)d_in[3], (const unsigned short*)d_in[4], (char*)d_ws);
  hipLaunchKernelGGL(k2_oproj, dim3(256), dim3(256), LDS2, stream, (char*)d_ws, (unsigned short*)d_out);
}
```

```cpp
#include <hip/hip_runtime.h>
#include <hip/hip_bf16.h>
#include <stdint.h>

namespace {

using bf16x8 = __attribute__((ext_vector_type(8))) short;
using s16x4 = __attribute__((ext_vector_type(4))) short;
using f32x16 = __attribute__((ext_vector_type(16))) float;
using f32x4 = __attribute__((ext_vector_type(4))) float;
using f32x2 = __attribute__((ext_vector_type(2))) float;
using u32x4 = __attribute__((ext_vector_type(4))) unsigned;
using u32x2 = __attribute__((ext_vector_type(2))) unsigned;
typedef short v4i16_t __attribute__((ext_vector_type(4)));
typedef __attribute__((address_space(3))) char* lds_ptr;
typedef __attribute__((address_space(3))) const char* lds_cptr;

#define DI __device__ __forceinline__
#define SBAR() __builtin_amdgcn_sched_barrier(0)
#define MFMA32(a, b, c) __builtin_amdgcn_mfma_f32_32x32x16_bf16((a), (b), (c), 0, 0, 0)
#define MFMA16(a, b, c) __builtin_amdgcn_mfma_f32_16x16x32_bf16((a), (b), (c), 0, 0, 0)

constexpr int NB = 8, SQ = 112, DM = 640, NH = 32, SK = 512, DH = 128;
constexpr int SLOT = 16384;
constexpr int LDS1 = 10 * SLOT;
constexpr float SCL = 0.08838834764831845f * 1.4426950408889634f;

constexpr size_t WS_REQ = 0;
constexpr size_t WS_ACK = 16384;
constexpr size_t WS_O = 32768;
constexpr size_t WS_O_BYTES = (size_t)NB * 128 * 7 * 1024;
constexpr size_t WS_PART = WS_O + WS_O_BYTES;
constexpr size_t WS_PART_BYTES = (size_t)32 * 8 * 70 * 1024;

#define STAMP(i) do {} while (0)
#define STAMP2(i) do {} while (0)
DI s16x4 vtr(lds_cptr p) {
  return __builtin_bit_cast(s16x4, __builtin_amdgcn_ds_read_tr16_b64_v4i16((__attribute__((address_space(3))) v4i16_t*)p));
}
DI bf16x8 ldsr128(lds_cptr p) { return *(const __attribute__((address_space(3))) bf16x8*)p; }
DI bf16x8 cat8(s16x4 lo, s16x4 hi) { return __builtin_shufflevector(lo, hi, 0, 1, 2, 3, 4, 5, 6, 7); }
typedef __bf16 bfv2 __attribute__((ext_vector_type(2)));
DI unsigned cvtpk(float lo, float hi) {
  const f32x2 v = {lo, hi};
  return __builtin_bit_cast(unsigned, __builtin_convertvector(v, bfv2));
}
template <int S>
DI bf16x8 pack8(const f32x16& x) {
  u32x4 p = {cvtpk(x[8 * S + 0], x[8 * S + 1]), cvtpk(x[8 * S + 2], x[8 * S + 3]), cvtpk(x[8 * S + 4], x[8 * S + 5]),
             cvtpk(x[8 * S + 6], x[8 * S + 7])};
  return __builtin_bit_cast(bf16x8, p);
}
template <bool NT>
DI void glds16(const void* sbase, unsigned voff, unsigned lds_dst) {
  unsigned sv;
  if constexpr (NT)
    asm volatile("s_mov_b32 %0, m0\n\ts_mov_b32 m0, %3\n\ts_nop 2\n\tglobal_load_lds_dwordx4 %1, %2 nt\n\ts_mov_b32 m0, %0"
                 : "=&s"(sv) : "v"(voff), "s"(sbase), "s"(lds_dst) : "memory");
  else
    asm volatile("s_mov_b32 %0, m0\n\ts_mov_b32 m0, %3\n\ts_nop 2\n\tglobal_load_lds_dwordx4 %1, %2\n\ts_mov_b32 m0, %0"
                 : "=&s"(sv) : "v"(voff), "s"(sbase), "s"(lds_dst) : "memory");
}
template <int N>
DI void wait_bar() {
  asm volatile("s_waitcnt vmcnt(%0) lgkmcnt(0)\n\ts_barrier" ::"i"(N) : "memory");
}
template <bool WT>
DI void store16(char* p, bf16x8 v) {
  if constexpr (WT) asm volatile("global_store_dwordx4 %0, %1, off sc1\n\ts_nop 1" ::"v"(p), "v"(v) : "memory");
  else *(bf16x8*)p = v;
}
#define WT_O false
#define WT_W true
DI void lds_bar() { asm volatile("s_waitcnt lgkmcnt(0)\n\ts_barrier" ::: "memory"); }

constexpr int sslot(int i) { return (2 * i) % 10; }
constexpr int kvslot(int it) { return it % 4; }
constexpr int XSLOT = 6;
constexpr int WO1 = 65536;

struct K1Ctx {
  const char* xb;
  const char* wqb;
  const char* kb;
  const char* vb;
  unsigned vx0, vx1, vw0, vw1, vk0, vk1, vv0, vv1;
  unsigned ldsw;
  int rot;
  const char* wob;
  unsigned lds0;
  int w, lane;
};

#define NTP false
#define ROTN 10
template <int I, int J>
DI void dma_s(const K1Ctx& c) {
  constexpr int sl = sslot(I);
  int st = I + c.rot;
  st = st >= 10 ? st - 10 : st;
  if constexpr (J == 0) glds16<NTP>(c.xb + st * 128, c.vx0, c.ldsw + sl * SLOT);
  if constexpr (J == 1) glds16<NTP>(c.wqb + (size_t)st * 64 * 8192, c.vw0, c.ldsw + (sl + 1) * SLOT);
  if constexpr (J == 2) glds16<NTP>(c.xb + st * 128, c.vx1, c.ldsw + sl * SLOT + 1024);
  if constexpr (J == 3) glds16<NTP>(c.wqb + (size_t)st * 64 * 8192, c.vw1, c.ldsw + (sl + 1) * SLOT + 1024);
}
constexpr bool pos_is_k(int n) { return n == 0 || ((n & 1) && n < 15); }
constexpr int pos_tile(int n) { return n == 0 ? 0 : (n == 15 ? 7 : ((n & 1) ? (n + 1) / 2 : n / 2 - 1)); }
template <int IT, int J>
DI void dma_kv(const K1Ctx& c) {
  if constexpr (pos_is_k(IT))
    glds16<true>(c.kb + pos_tile(IT) * 16384, J ? c.vk1 : c.vk0, c.ldsw + kvslot(IT) * SLOT + J * 1024);
  else
    glds16<true>(c.vb + pos_tile(IT) * 16384, J ? c.vv1 : c.vv0, c.ldsw + kvslot(IT) * SLOT + J * 1024);
}
template <int IT>
DI void dma_kv2(const K1Ctx& c) { dma_kv<IT, 0>(c); dma_kv<IT, 1>(c); }
constexpr int RA = 3;
template <int I, int J>
DI void dma_in_stage(const K1Ctx& c) {
  if constexpr (I <= 6) dma_s<I + 3, J>(c);
  else if constexpr ((I - 7) * 2 + J / 2 < RA) dma_kv<(I - 7) * 2 + J / 2, J % 2>(c);
}
template <int N>
DI void wait_vm() { asm volatile("s_waitcnt vmcnt(%0)" ::"i"(N) : "memory"); }

struct PFrag { bf16x8 x, a0, a1; };
template <int I, int KS>
DI PFrag proj_load(lds_cptr lds, unsigned xoff, unsigned woff0, unsigned woff1) {
  lds_cptr xs = lds + sslot(I) * SLOT, ws = lds + (sslot(I) + 1) * SLOT;
  PFrag f;
  f.x = ldsr128(xs + (xoff ^ (unsigned)(KS * 32)));
  f.a0 = cat8(vtr(ws + woff0 + KS * 4096), vtr(ws + woff0 + KS * 4096 + 1024));
  f.a1 = cat8(vtr(ws + woff1 + KS * 4096), vtr(ws + woff1 + KS * 4096 + 1024));
  return f;
}
#define PMFMA(a, b, c) MFMA32(a, b, c)
template <int I>
DI void proj_stage(const K1Ctx& c, lds_cptr lds, unsigned xoff, unsigned woff0, unsigned woff1, f32x16& q0, f32x16& q1,
                   PFrag& f0, PFrag& f1) {
  PFrag f2 = proj_load<I, 2>(lds, xoff, woff0, woff1);
  SBAR();
  q0 = PMFMA(f0.a0, f0.x, q0);
  q1 = PMFMA(f0.a1, f0.x, q1);
  SBAR();
  dma_in_stage<I, 0>(c);
  PFrag f3 = proj_load<I, 3>(lds, xoff, woff0, woff1);
  SBAR();
  q0 = PMFMA(f1.a0, f1.x, q0);
  q1 = PMFMA(f1.a1, f1.x, q1);
  SBAR();
  dma_in_stage<I, 1>(c);
  if constexpr (I < 9) f0 = proj_load<I + 1, 0>(lds, xoff, woff0, woff1);
  SBAR();
  q0 = PMFMA(f2.a0, f2.x, q0);
  q1 = PMFMA(f2.a1, f2.x, q1);
  SBAR();
  dma_in_stage<I, 2>(c);
  if constexpr (I < 9) f1 = proj_load<I + 1, 1>(lds, xoff, woff0, woff1);
  SBAR();
  q0 = PMFMA(f3.a0, f3.x, q0);
  q1 = PMFMA(f3.a1, f3.x, q1);
  SBAR();
  dma_in_stage<I, 3>(c);
  if constexpr (I <= 7) wait_bar<4>();
  else if constexpr (I == 8) lds_bar();
}

DI float max16(const f32x16& s) {
  float a = fmaxf(fmaxf(s[0], s[1]), s[2]), b = fmaxf(fmaxf(s[3], s[4]), s[5]);
  a = fmaxf(fmaxf(a, s[6]), s[7]);
  b = fmaxf(fmaxf(b, s[8]), s[9]);
  a = fmaxf(fmaxf(a, s[10]), s[11]);
  b = fmaxf(fmaxf(b, s[12]), s[13]);
  a = fmaxf(fmaxf(a, s[14]), s[15]);
  return fmaxf(a, b);
}
DI float swap32(float v) {
  auto r = __builtin_amdgcn_permlane32_swap(__float_as_uint(v), __float_as_uint(v), false, false);
  return (threadIdx.x & 32) ? __uint_as_float(r[0]) : __uint_as_float(r[1]);
}
DI float max_halves(float v) {
  auto r = __builtin_amdgcn_permlane32_swap(__float_as_uint(v), __float_as_uint(v), false, false);
  return fmaxf(__uint_as_float(r[0]), __uint_as_float(r[1]));
}
DI float sum_halves(float v) {
  auto r = __builtin_amdgcn_permlane32_swap(__float_as_uint(v), __float_as_uint(v), false, false);
  return __uint_as_float(r[0]) + __uint_as_float(r[1]);
}
constexpr float THR_LOG2 = 16.f;
constexpr float THR_RAW = THR_LOG2 / SCL;
struct SmState { float mref, l, fs, nmc; bool moved; };

DI void dma_wo(const K1Ctx& c, int gi) {
  const int pb = gi / 5, e = gi - 5 * pb;
  const unsigned ch = (unsigned)(64 * e + c.lane);
  const unsigned row = (ch * 205u) >> 11;
  glds16<true>(c.wob + (size_t)pb * 32 * 1280, row * 1280u + (ch - 10u * row) * 16u, c.lds0 + WO1 + 1024u * (unsigned)gi);
}
constexpr int kv_extra(int s) { return s >= 0 && s <= 9 ? 1 : 0; }
constexpr int kv_wait(int n) {
  return (n + 1 <= 15 ? 2 : 0) + (n + 2 <= 15 ? 2 : 0) + kv_extra(n - 3) + kv_extra(n - 2) + kv_extra(n - 1);
}
template <int N>
DI void step_sync(const K1Ctx& c) {
  wait_bar<kv_wait(N)>();
  if constexpr (N + 3 <= 15) dma_kv2<N + 3>(c);
  if constexpr (N <= 9) dma_wo(c, c.w + 8 * N);
}
template <bool FIRST>
DI void sm_decide(const f32x16& s, SmState& st) {
  const float tm = max_halves(max16(s));
  if constexpr (FIRST) {
    st.mref = tm;
    st.fs = 1.f;
    st.moved = false;
  } else {
    st.fs = 1.f;
    st.moved = false;
    if (__builtin_expect(__any(tm - st.mref > THR_RAW), 0)) {
      const float mn = fmaxf(st.mref, tm);
      st.fs = __builtin_amdgcn_exp2f((st.mref - mn) * SCL);
      st.mref = mn;
      st.moved = true;
    }
  }
  st.nmc = -st.mref * SCL;
}
DI unsigned sm_pair(const f32x16& s, int i, float nmc, float& la, float& lb) {
  const float e0 = __builtin_amdgcn_exp2f(__builtin_fmaf(s[2 * i], SCL, nmc));
  const float e1 = __builtin_amdgcn_exp2f(__builtin_fmaf(s[2 * i + 1], SCL, nmc));
  la += e0;
  lb += e1;
  asm volatile("" : "+v"(la), "+v"(lb));
  return cvtpk(e0, e1);
}
template <int N, bool SM>
DI void k_step(const K1Ctx& c, lds_cptr lds, unsigned koff, const bf16x8 (&qf)[8], f32x16& sn, const f32x16& so,
               SmState& st, u32x4& p0, u32x4& p1) {
  step_sync<N>(c);
  unsigned kb = koff + kvslot(N) * SLOT;
  asm volatile("" : "+v"(kb));
  bf16x8 kf[8];
#pragma unroll
  for (int ks = 0; ks < 8; ++ks) kf[ks] = ldsr128(lds + (kb ^ (unsigned)(ks * 32)));
  float la = 0.f, lb = 0.f;
  const float nmc = st.nmc;
  sn = f32x16{};
  SBAR();
#pragma unroll
  for (int ks = 0; ks < 8; ++ks) {
    sn = MFMA32(kf[ks], qf[ks], sn);
    if constexpr (SM) {
      const unsigned wd = sm_pair(so, ks, nmc, la, lb);
      if (ks < 4) p0[ks] = wd; else p1[ks - 4] = wd;
    }
    SBAR();
  }
  if constexpr (SM) st.l = st.l * st.fs + (la + lb);
}
template <int N, bool SM>
DI void v_step(const K1Ctx& c, lds_cptr lds, const unsigned (&voff)[4], const u32x4& p0, const u32x4& p1, float fs,
               bool moved, f32x16 (&o)[4], const f32x16& so, SmState& st, u32x4& q0, u32x4& q1) {
  step_sync<N>(c);
  unsigned vb = voff[0] + kvslot(N) * SLOT;
  asm volatile("" : "+v"(vb));
  bf16x8 vf[8];
#pragma unroll
  for (int dt = 0; dt < 4; ++dt) {
    lds_cptr vp = lds + (vb ^ (unsigned)(dt * 64));
    vf[2 * dt] = cat8(vtr(vp), vtr(vp + 2048));
    vf[2 * dt + 1] = cat8(vtr(vp + 4096), vtr(vp + 4096 + 2048));
  }
  if (__builtin_expect(moved, 0)) {
#pragma unroll
    for (int dt = 0; dt < 4; ++dt) o[dt] *= fs;
  }
  float la = 0.f, lb = 0.f;
  const float nmc = st.nmc;
  SBAR();
#pragma unroll
  for (int i = 0; i < 8; ++i) {
    o[i >> 1] = MFMA32(vf[i], __builtin_bit_cast(bf16x8, (i & 1) ? p1 : p0), o[i >> 1]);
    if constexpr (SM) {
      const unsigned wd = sm_pair(so, i, nmc, la, lb);
      if (i < 4) q0[i] = wd; else q1[i - 4] = wd;
    }
    SBAR();
  }
  if constexpr (SM) st.l = st.l * st.fs + (la + lb);
}

DI unsigned ld_sc1(const char* p) { return __hip_atomic_load((const unsigned*)p, __ATOMIC_RELAXED, __HIP_MEMORY_SCOPE_AGENT); }
DI void st_sc1(char* p, unsigned v) { __hip_atomic_store((unsigned*)p, v, __ATOMIC_RELAXED, __HIP_MEMORY_SCOPE_AGENT); }

template <int LO, int NP>
DI void handoff(char* ws, int lane, unsigned my_off, unsigned peer_off, unsigned r, unsigned v) {
  const bool live = lane >= LO && lane < LO + NP;
  if (live) st_sc1(ws + WS_ACK + peer_off, v);
  for (int spin = 0; spin < (1 << 22); ++spin) {
    const unsigned a = ld_sc1(ws + WS_ACK + my_off);
    const unsigned vq = ld_sc1(ws + WS_REQ + my_off);
    if (live && vq != v) st_sc1(ws + WS_ACK + peer_off, vq);
    v = vq;
    if (__all(!live || a == r)) break;
    __builtin_amdgcn_s_sleep(1);
  }
  const unsigned vq = ld_sc1(ws + WS_REQ + my_off);
  if (live && vq != v) st_sc1(ws + WS_ACK + peer_off, vq);
}

__global__ __launch_bounds__(512, 2) void gqa_fused(const char* __restrict__ x, const char* __restrict__ kc,
                                                     const char* __restrict__ vc, const char* __restrict__ wq,
                                                     const char* __restrict__ wo, char* __restrict__ ws,
                                                     unsigned short* __restrict__ out) {
  extern __shared__ __attribute__((aligned(16))) char lds_raw[];
  const lds_ptr lds = (lds_ptr)lds_raw;
  const int tid = threadIdx.x, lane = tid & 63;
  const int w = __builtin_amdgcn_readfirstlane(tid >> 6);
  const int qt = w & 3, g = w >> 2;
  const int bid = blockIdx.x, xcd = bid & 7, idx = bid >> 3;
  const int h = (xcd & 3) * 8 + (idx & 7), b = (xcd >> 2) * 4 + (idx >> 3);
  const int k8 = h >> 2, bp = b >> 1, nt = (b & 1) * 4 + (h & 3);
  const int r32 = lane & 31, hh = lane >> 5, blk = (lane >> 4) & 1, q4 = (lane & 15) >> 2, p = lane & 3;

  STAMP(0);
  const int sl = lane & 15;
  const unsigned my_off = (unsigned)((b * 32 + h) * 64 + 4 * sl);
  const unsigned peer_off = sl < 8 ? (unsigned)(((2 * bp + (sl >> 2)) * 32 + 4 * k8 + (sl & 3)) * 64 + 4 * nt)
                                   : (unsigned)((b * 32 + 4 * (sl - 8) + (h & 3)) * 64 + 32 + 4 * k8);
  unsigned rq = 0;
  if (w == 7) asm volatile("global_load_dword %0, %1, %2 sc1" : "=v"(rq) : "v"(my_off), "s"(ws + WS_ACK) : "memory");

  K1Ctx c;
  c.xb = x + (size_t)b * SQ * DM * 2;
  c.wqb = wq + h * 256;
  c.kb = kc + (size_t)(b * NH + h) * SK * DH * 2;
  c.vb = vc + (size_t)(b * NH + h) * SK * DH * 2;
  c.wob = wo + (size_t)k8 * 512 * 1280 + nt * 160;
  c.w = w;
  c.lane = lane;
  {
    const int xr0 = 16 * w + (lane >> 3), xr1 = xr0 + 8;
    c.vx0 = xr0 < SQ ? (unsigned)(xr0 * 1280 + (((lane & 7) ^ ((xr0 >> 1) & 7)) << 4)) : 0u;
    c.vx1 = xr1 < SQ ? (unsigned)(xr1 * 1280 + (((lane & 7) ^ ((xr1 >> 1) & 7)) << 4)) : 0u;
    const int r0 = 8 * w + (lane >> 4), r1 = r0 + 4;
    c.vw0 = (unsigned)(r0 * 8192 + (((lane & 15) ^ ((r0 & 3) << 2)) << 4));
    c.vw1 = (unsigned)(r1 * 8192 + (((lane & 15) ^ ((r1 & 3) << 2)) << 4));
    c.vk0 = (unsigned)(r0 * 256 + (((lane & 15) ^ (r0 & 15)) << 4));
    c.vk1 = (unsigned)(r1 * 256 + (((lane & 15) ^ (r1 & 15)) << 4));
    c.vv0 = (unsigned)(r0 * 256 + (((lane & 15) ^ ((r0 & 3) << 2)) << 4));
    c.vv1 = (unsigned)(r1 * 256 + (((lane & 15) ^ ((r1 & 3) << 2)) << 4));
  }
  c.lds0 = (unsigned)(uintptr_t)lds_raw;
  c.ldsw = c.lds0 + (unsigned)w * 2048u;
  c.rot = (((b & 3) * 5) / 2 + ((h & 7) * 5) / 4) % 10;

  dma_s<0, 0>(c); dma_s<0, 1>(c); dma_s<0, 2>(c); dma_s<0, 3>(c);
  dma_s<1, 0>(c); dma_s<1, 1>(c); dma_s<1, 2>(c); dma_s<1, 3>(c);
  dma_s<2, 0>(c); dma_s<2, 1>(c); dma_s<2, 2>(c); dma_s<2, 3>(c);

  f32x16 q0 = {}, q1 = {};
  {
    const int xrow = 32 * qt + r32;
    const unsigned xoff = (unsigned)(xrow * 128 + ((hh ^ ((xrow >> 1) & 7)) << 4));
    const int sp = ((p & 1) << 1) | (p >> 1);
    const int d0 = 2 * g, d1 = 2 * g + 1;
    const unsigned wrow = (unsigned)((8 * hh + q4) * 256 + 8 * (sp & 1));
    const unsigned woff0 = wrow + (unsigned)((4 * (d0 ^ q4) + 2 * blk + (sp >> 1)) << 4);
    const unsigned woff1 = wrow + (unsigned)((4 * (d1 ^ q4) + 2 * blk + (sp >> 1)) << 4);
    wait_bar<4>();
    asm volatile("" : "+v"(rq));
    rq += 1u;
    if (w == 7 && lane < 16) st_sc1(ws + WS_REQ + peer_off, rq);
    PFrag f0 = proj_load<0, 0>(lds, xoff, woff0, woff1);
    PFrag f1 = proj_load<0, 1>(lds, xoff, woff0, woff1);
    proj_stage<0>(c, lds, xoff, woff0, woff1, q0, q1, f0, f1);
    STAMP(1);
    proj_stage<1>(c, lds, xoff, woff0, woff1, q0, q1, f0, f1);
    proj_stage<2>(c, lds, xoff, woff0, woff1, q0, q1, f0, f1);
    proj_stage<3>(c, lds, xoff, woff0, woff1, q0, q1, f0, f1);
    proj_stage<4>(c, lds, xoff, woff0, woff1, q0, q1, f0, f1);
    proj_stage<5>(c, lds, xoff, woff0, woff1, q0, q1, f0, f1);
    proj_stage<6>(c, lds, xoff, woff0, woff1, q0, q1, f0, f1);
    proj_stage<7>(c, lds, xoff, woff0, woff1, q0, q1, f0, f1);
    proj_stage<8>(c, lds, xoff, woff0, woff1, q0, q1, f0, f1);
    proj_stage<9>(c, lds, xoff, woff0, woff1, q0, q1, f0, f1);
    STAMP(2);
  }
  bf16x8 qf[8];
  {
    lds_ptr e = lds + XSLOT * SLOT + qt * 8192 + g * 4096 + lane * 16;
    *(__attribute__((address_space(3))) bf16x8*)(e) = pack8<0>(q0);
    *(__attribute__((address_space(3))) bf16x8*)(e + 1024) = pack8<1>(q0);
    *(__attribute__((address_space(3))) bf16x8*)(e + 2048) = pack8<0>(q1);
    *(__attribute__((address_space(3))) bf16x8*)(e + 3072) = pack8<1>(q1);
  }
  lds_bar();
  {
    lds_cptr e = lds + XSLOT * SLOT + qt * 8192 + lane * 16;
#pragma unroll
    for (int ks = 0; ks < 8; ++ks) qf[ks] = ldsr128(e + ks * 1024);
  }
  STAMP(3);
  f32x16 o[4] = {};
  SmState st;
  st.l = 0.f;
  float m, l;
  {
    const int krow = 32 * g + r32;
    const unsigned koff = (unsigned)(krow * 256 + ((hh ^ (krow & 15)) << 4));
    unsigned voff[4];
    const unsigned vrow = (unsigned)((32 * g + 4 * hh + q4) * 256 + 8 * (p & 1));
#pragma unroll
    for (int dt = 0; dt < 4; ++dt) voff[dt] = vrow + (unsigned)((4 * (dt ^ q4) + 2 * blk + (p >> 1)) << 4);
    f32x16 sA, sB;
    u32x4 pa0, pa1, pb0, pb1;
    float fsp; bool mvp;
#define KSTEP(N, SN, SO, P0, P1)                                   \
    fsp = st.fs; mvp = st.moved;                                   \
    k_step<N, true>(c, lds, koff, qf, SN, SO, st, P0, P1);         \
    sm_decide<false>(SN, st);
#define VSTEP(N, P0, P1) v_step<N, false>(c, lds, voff, P0, P1, fsp, mvp, o, sA, st, pb0, pb1);
    k_step<0, false>(c, lds, koff, qf, sA, sA, st, pa0, pa1);
    sm_decide<true>(sA, st);
    KSTEP(1, sB, sA, pa0, pa1)   VSTEP(2, pa0, pa1)
    KSTEP(3, sA, sB, pa0, pa1)   VSTEP(4, pa0, pa1)
    KSTEP(5, sB, sA, pa0, pa1)   VSTEP(6, pa0, pa1)
    KSTEP(7, sA, sB, pa0, pa1)   VSTEP(8, pa0, pa1)
    KSTEP(9, sB, sA, pa0, pa1)   VSTEP(10, pa0, pa1)
    KSTEP(11, sA, sB, pa0, pa1)  VSTEP(12, pa0, pa1)
    KSTEP(13, sB, sA, pa0, pa1)
    {
      const float fs7 = st.fs; const bool mv7 = st.moved;
      v_step<14, true>(c, lds, voff, pa0, pa1, fsp, mvp, o, sB, st, pb0, pb1);
      m = st.mref;
      l = sum_halves(st.l);
      *(__attribute__((address_space(3))) f32x2*)(lds + w * 512 + lane * 8) = f32x2{m, l};
      v_step<15, false>(c, lds, voff, pb0, pb1, fs7, mv7, o, sA, st, pa0, pa1);
    }
#undef KSTEP
#undef VSTEP
  }
  STAMP(4);
  {
    const int w2 = w ^ 4;
    const f32x2 ml2 = *(const __attribute__((address_space(3))) f32x2*)(lds + w2 * 512 + lane * 8);
    lds_bar();
    lds_ptr mb = lds + w * 8192 + lane * 16;
    f32x16 sa, sb, ka, kb2;
    if (g == 0) { sa = o[2]; sb = o[3]; ka = o[0]; kb2 = o[1]; }
    else        { sa = o[0]; sb = o[1]; ka = o[2]; kb2 = o[3]; }
#pragma unroll
    for (int i = 0; i < 4; ++i) {
      *(__attribute__((address_space(3))) f32x4*)(mb + i * 1024) = f32x4{sa[4 * i], sa[4 * i + 1], sa[4 * i + 2], sa[4 * i + 3]};
      *(__attribute__((address_space(3))) f32x4*)(mb + 4096 + i * 1024) = f32x4{sb[4 * i], sb[4 * i + 1], sb[4 * i + 2], sb[4 * i + 3]};
    }
    const float mt = fmaxf(m, ml2[0]);
    const float a1 = __builtin_amdgcn_exp2f((m - mt) * SCL), a2 = __builtin_amdgcn_exp2f((ml2[0] - mt) * SCL);
    const float inv = 1.0f / (a1 * l + a2 * ml2[1]);
    const float f1 = a1 * inv, f2 = a2 * inv;
    lds_bar();
    lds_cptr pb = lds + w2 * 8192 + lane * 16;
#pragma unroll
    for (int i = 0; i < 4; ++i) {
      const f32x4 u0 = *(const __attribute__((address_space(3))) f32x4*)(pb + i * 1024);
      const f32x4 u1 = *(const __attribute__((address_space(3))) f32x4*)(pb + 4096 + i * 1024);
#pragma unroll
      for (int j = 0; j < 4; ++j) {
        ka[4 * i + j] = ka[4 * i + j] * f1 + u0[j] * f2;
        kb2[4 * i + j] = kb2[4 * i + j] * f1 + u1[j] * f2;
      }
    }
    const int m16 = 2 * qt + (r32 >> 4);
    if (m16 < 7) {
      char* ob = ws + WS_O + ((size_t)((b * 128 + h * 4 + 2 * g) * 7 + m16)) * 1024 + (16 * hh + (r32 & 15)) * 16;
      auto pk = [](const f32x16& v, int u) {
        u32x4 t = {cvtpk(v[4 * u], v[4 * u + 1]), cvtpk(v[4 * u + 2], v[4 * u + 3]), cvtpk(v[4 * u + 8], v[4 * u + 9]),
                   cvtpk(v[4 * u + 10], v[4 * u + 11])};
        return __builtin_bit_cast(bf16x8, t);
      };
      store16<true>(ob, pk(ka, 0));
      store16<true>(ob + 512, pk(ka, 1));
      store16<true>(ob + 7 * 1024, pk(kb2, 0));
      store16<true>(ob + 7 * 1024 + 512, pk(kb2, 1));
    }
  }
  unsigned rv = 0;
  if (w == 7) rv = ld_sc1(ws + WS_REQ + my_off);
  wait_bar<0>();
  STAMP(5);
  if (w == 7) handoff<0, 8>(ws, lane, my_off, peer_off, rq, rv);
  wait_bar<0>();
  STAMP(6);
  const int gg = lane >> 4;
  const auto prs = __builtin_amdgcn_make_buffer_rsrc((void*)(ws + WS_PART + (size_t)(bp * 8 + nt) * 8 * 70 * 1024), 0,
                                                     8 * 70 * 1024, 0x00020000);
  f32x4 acc[2][5];
#pragma unroll
  for (int i = 0; i < 2; ++i)
#pragma unroll
    for (int j = 0; j < 5; ++j) acc[i][j] = f32x4{0.f, 0.f, 0.f, 0.f};
  if (w < 7) {
    const auto ors = __builtin_amdgcn_make_buffer_rsrc((void*)(ws + WS_O + (size_t)(2 * bp * 128 + 16 * k8) * 7 * 1024), 0,
                                                       (128 + 16) * 7 * 1024, 0x00020000);
    const int mo = w * 1024 + lane * 16;
    const unsigned wl = (unsigned)(WO1 + (4 * gg + q4) * 160 + 8 * p);
    constexpr int PD = 4;
    const int rot = 2 * nt;
    u32x4 of[PD][2];
#pragma unroll
    for (int ks = 0; ks < PD; ++ks) {
      of[ks][0] = __builtin_amdgcn_raw_buffer_load_b128(ors, mo, ((ks + rot) & 15) * 7168, 16);
      of[ks][1] = __builtin_amdgcn_raw_buffer_load_b128(ors, mo, (128 + ((ks + rot) & 15)) * 7168, 16);
    }
    SBAR();
#pragma unroll
    for (int ks = 0; ks < 16; ++ks) {
      lds_cptr wp = lds + (wl + (unsigned)(((ks + rot) & 15) * 5120));
      bf16x8 wf[5];
#pragma unroll
      for (int j = 0; j < 5; ++j) wf[j] = cat8(vtr(wp + 32 * j), vtr(wp + 32 * j + 2560));
      const bf16x8 o0 = __builtin_bit_cast(bf16x8, of[ks % PD][0]), o1 = __builtin_bit_cast(bf16x8, of[ks % PD][1]);
#pragma unroll
      for (int j = 0; j < 5; ++j) {
        acc[0][j] = MFMA16(wf[j], o0, acc[0][j]);
        acc[1][j] = MFMA16(wf[j], o1, acc[1][j]);
      }
      if (ks + PD < 16) {
        of[ks % PD][0] = __builtin_amdgcn_raw_buffer_load_b128(ors, mo, ((ks + PD + rot) & 15) * 7168, 16);
        of[ks % PD][1] = __builtin_amdgcn_raw_buffer_load_b128(ors, mo, (128 + ((ks + PD + rot) & 15)) * 7168, 16);
      }
      SBAR();
    }
    STAMP(7);
#pragma unroll
    for (int i = 0; i < 2; ++i) {
      if (!(gg == (k8 & 3) && i == (k8 >> 2))) {
#pragma unroll
        for (int j = 0; j < 5; ++j)
          __builtin_amdgcn_raw_buffer_store_b128(__builtin_bit_cast(u32x4, acc[i][j]), prs,
                                                 (k8 * 70 + (i * 7 + w) * 5 + j) * 1024 + lane * 16, 0, 16);
      }
    }
  } else {
    rv = ld_sc1(ws + WS_REQ + my_off);
  }
  wait_bar<0>();
  STAMP(8);
  if (w == 7) handoff<8, 8>(ws, lane, my_off, peer_off, rq, rv);
  wait_bar<0>();
  STAMP(9);
  if (w < 7) {
    const int ih = k8 >> 2;
    const int po = ((ih * 7 + w) * 5) * 1024 + (16 * (k8 & 3) + (lane & 15)) * 16;
    u32x4 u[2][5];
#pragma unroll
    for (int j = 0; j < 5; ++j) {
      u[0][j] = __builtin_amdgcn_raw_buffer_load_b128(prs, gg * 70 * 1024 + po + j * 1024, 0, 16);
      u[1][j] = __builtin_amdgcn_raw_buffer_load_b128(prs, (gg + 4) * 70 * 1024 + po + j * 1024, 0, 16);
    }
    f32x4 r[5];
#pragma unroll
    for (int j = 0; j < 5; ++j) {
      const f32x4 own = ih ? acc[1][j] : acc[0][j];
      const f32x4 a = gg == k8 ? own : __builtin_bit_cast(f32x4, u[0][j]);
      const f32x4 bq = gg + 4 == k8 ? own : __builtin_bit_cast(f32x4, u[1][j]);
      f32x4 t = a + bq;
#pragma unroll
      for (int e = 0; e < 4; ++e) {
        t[e] += __shfl_xor(t[e], 16);
        t[e] += __shfl_xor(t[e], 32);
      }
      r[j] = t;
    }
    const f32x4 t0 = gg == 0 ? r[0] : gg == 1 ? r[1] : gg == 2 ? r[2] : r[3];
    unsigned short* op = out + (size_t)((2 * bp + ih) * SQ + 16 * w + (lane & 15)) * DM + 80 * nt + 4 * (k8 & 3);
    *(u32x2*)(op + 16 * gg) = u32x2{cvtpk(t0[0], t0[1]), cvtpk(t0[2], t0[3])};
    if (gg == 0) *(u32x2*)(op + 64) = u32x2{cvtpk(r[4][0], r[4][1]), cvtpk(r[4][2], r[4][3])};
  }
}

}

extern "C" void kernel_launch(void* const* d_in, const int* in_sizes, int n_in, void* d_out, int out_size, void* d_ws,
                              size_t ws_size, hipStream_t stream) {
  (void)in_sizes; (void)n_in; (void)out_size; (void)ws_size;
  (void)hipFuncSetAttribute((const void*)gqa_fused, hipFuncAttributeMaxDynamicSharedMemorySize, LDS1);
  hipLaunchKernelGGL(gqa_fused, dim3(256), dim3(512), LDS1, stream, (const char*)d_in[0], (const char*)d_in[1],
                     (const char*)d_in[2], (const char*)d_in[3], (const char*)d_in[4], (char*)d_ws,
                     (unsigned short*)d_out);
}
```

```cpp
#include <hip/hip_runtime.h>
#include <hip/hip_bf16.h>
#include <stdint.h>

namespace {

using bf16x8 = __attribute__((ext_vector_type(8))) short;
using s16x4 = __attribute__((ext_vector_type(4))) short;
using f32x16 = __attribute__((ext_vector_type(16))) float;
using f32x4 = __attribute__((ext_vector_type(4))) float;
using f32x2 = __attribute__((ext_vector_type(2))) float;
using u32x4 = __attribute__((ext_vector_type(4))) unsigned;
using u32x2 = __attribute__((ext_vector_type(2))) unsigned;
typedef short v4i16_t __attribute__((ext_vector_type(4)));
typedef __attribute__((address_space(3))) char* lds_ptr;
typedef __attribute__((address_space(3))) const char* lds_cptr;

#define DI __device__ __forceinline__
#define SBAR() __builtin_amdgcn_sched_barrier(0)
#define MFMA32(a, b, c) __builtin_amdgcn_mfma_f32_32x32x16_bf16((a), (b), (c), 0, 0, 0)
#define MFMA16(a, b, c) __builtin_amdgcn_mfma_f32_16x16x32_bf16((a), (b), (c), 0, 0, 0)

constexpr int NB = 8, SQ = 112, DM = 640, NH = 32, SK = 512, DH = 128;
constexpr int SLOT = 16384;
constexpr int LDS1 = 10 * SLOT;
constexpr float SCL = 0.08838834764831845f * 1.4426950408889634f;

constexpr size_t WS_REQ = 0;
constexpr size_t WS_ACK = 16384;
constexpr size_t WS_O = 32768;
constexpr size_t WS_O_BYTES = (size_t)NB * 128 * 7 * 1024;
constexpr size_t WS_PART = WS_O + WS_O_BYTES;
constexpr size_t WS_PART_BYTES = (size_t)32 * 8 * 70 * 1024;

DI s16x4 vtr(lds_cptr p) {
  return __builtin_bit_cast(s16x4, __builtin_amdgcn_ds_read_tr16_b64_v4i16((__attribute__((address_space(3))) v4i16_t*)p));
}
DI bf16x8 ldsr128(lds_cptr p) { return *(const __attribute__((address_space(3))) bf16x8*)p; }
DI bf16x8 cat8(s16x4 lo, s16x4 hi) { return __builtin_shufflevector(lo, hi, 0, 1, 2, 3, 4, 5, 6, 7); }
typedef __bf16 bfv2 __attribute__((ext_vector_type(2)));
DI unsigned cvtpk(float lo, float hi) {
  const f32x2 v = {lo, hi};
  return __builtin_bit_cast(unsigned, __builtin_convertvector(v, bfv2));
}
template <int S>
DI bf16x8 pack8(const f32x16& x) {
  u32x4 p = {cvtpk(x[8 * S + 0], x[8 * S + 1]), cvtpk(x[8 * S + 2], x[8 * S + 3]), cvtpk(x[8 * S + 4], x[8 * S + 5]),
             cvtpk(x[8 * S + 6], x[8 * S + 7])};
  return __builtin_bit_cast(bf16x8, p);
}
template <bool NT>
DI void glds16(const void* sbase, unsigned voff, unsigned lds_dst) {
  unsigned sv;
  if constexpr (NT)
    asm volatile("s_mov_b32 %0, m0\n\ts_mov_b32 m0, %3\n\ts_nop 2\n\tglobal_load_lds_dwordx4 %1, %2 nt\n\ts_mov_b32 m0, %0"
                 : "=&s"(sv) : "v"(voff), "s"(sbase), "s"(lds_dst) : "memory");
  else
    asm volatile("s_mov_b32 %0, m0\n\ts_mov_b32 m0, %3\n\ts_nop 2\n\tglobal_load_lds_dwordx4 %1, %2\n\ts_mov_b32 m0, %0"
                 : "=&s"(sv) : "v"(voff), "s"(sbase), "s"(lds_dst) : "memory");
}
template <int N>
DI void wait_bar() {
  asm volatile("s_waitcnt vmcnt(%0) lgkmcnt(0)\n\ts_barrier" ::"i"(N) : "memory");
}
DI void store_wt(char* p, bf16x8 v) {
  asm volatile("global_store_dwordx4 %0, %1, off sc1\n\ts_nop 1" ::"v"(p), "v"(v) : "memory");
}
DI void lds_bar() { asm volatile("s_waitcnt lgkmcnt(0)\n\ts_barrier" ::: "memory"); }

constexpr int sslot(int i) { return (2 * i) % 10; }
constexpr int kvaddr(int it) { return it % 5 < 4 ? (it % 5) * SLOT : 9 * SLOT; }
constexpr int XSLOT = 6;
constexpr int WO1 = 65536;

struct K1Ctx {
  const char* xb;
  const char* wqb;
  const char* kb;
  const char* vb;
  unsigned vx0, vx1, vw0, vw1, vk0, vk1, vv0, vv1;
  unsigned ldsw;
  int rot;
  const char* wob;
  unsigned lds0;
  int w, lane;
};

template <int I, int J>
DI void dma_s(const K1Ctx& c) {
  constexpr int sl = sslot(I);
  int st = I + c.rot;
  st = st >= 10 ? st - 10 : st;
  if constexpr (J == 0) glds16<false>(c.xb + st * 128, c.vx0, c.ldsw + sl * SLOT);
  if constexpr (J == 1) glds16<false>(c.wqb + (size_t)st * 64 * 8192, c.vw0, c.ldsw + (sl + 1) * SLOT);
  if constexpr (J == 2) glds16<false>(c.xb + st * 128, c.vx1, c.ldsw + sl * SLOT + 1024);
  if constexpr (J == 3) glds16<false>(c.wqb + (size_t)st * 64 * 8192, c.vw1, c.ldsw + (sl + 1) * SLOT + 1024);
}
constexpr bool pos_is_k(int n) { return n == 0 || ((n & 1) && n < 15); }
constexpr int pos_tile(int n) { return n == 0 ? 0 : (n == 15 ? 7 : ((n & 1) ? (n + 1) / 2 : n / 2 - 1)); }
template <int IT, int J>
DI void dma_kv(const K1Ctx& c) {
  if constexpr (pos_is_k(IT))
    glds16<true>(c.kb + pos_tile(IT) * 16384, J ? c.vk1 : c.vk0, c.ldsw + kvaddr(IT) + J * 1024);
  else
    glds16<true>(c.vb + pos_tile(IT) * 16384, J ? c.vv1 : c.vv0, c.ldsw + kvaddr(IT) + J * 1024);
}
template <int IT>
DI void dma_kv2(const K1Ctx& c) { dma_kv<IT, 0>(c); dma_kv<IT, 1>(c); }
constexpr int RA = 4;
constexpr int PDIST = 3;
template <int I, int J>
DI void dma_in_stage(const K1Ctx& c) {
  if constexpr (I + PDIST <= 9) dma_s<I + PDIST, J>(c);
  else if constexpr ((I - 7) * 2 + J / 2 < RA) dma_kv<(I - 7) * 2 + J / 2, J % 2>(c);
}
constexpr int proj_wait(int I) {
  int n = 0;
  for (int st = I + 3; st <= I + PDIST && st <= 9; ++st) n += 4;
  if (I == 7) n += 4;
  return n;
}
template <int N>
DI void wait_vm() { asm volatile("s_waitcnt vmcnt(%0)" ::"i"(N) : "memory"); }

struct PFrag { bf16x8 x, a0, a1; };
template <int I, int KS>
DI PFrag proj_load(lds_cptr lds, unsigned xoff, unsigned woff0, unsigned woff1) {
  lds_cptr xs = lds + sslot(I) * SLOT, ws = lds + (sslot(I) + 1) * SLOT;
  PFrag f;
  f.x = ldsr128(xs + (xoff ^ (unsigned)(KS * 32)));
  f.a0 = cat8(vtr(ws + woff0 + KS * 4096), vtr(ws + woff0 + KS * 4096 + 1024));
  f.a1 = cat8(vtr(ws + woff1 + KS * 4096), vtr(ws + woff1 + KS * 4096 + 1024));
  return f;
}
template <int I>
DI void proj_stage(const K1Ctx& c, lds_cptr lds, unsigned xoff, unsigned woff0, unsigned woff1, f32x16& q0, f32x16& q1,
                   PFrag& f0, PFrag& f1) {
  PFrag f2 = proj_load<I, 2>(lds, xoff, woff0, woff1);
  SBAR();
  q0 = MFMA32(f0.a0, f0.x, q0);
  q1 = MFMA32(f0.a1, f0.x, q1);
  SBAR();
  dma_in_stage<I, 0>(c);
  PFrag f3 = proj_load<I, 3>(lds, xoff, woff0, woff1);
  SBAR();
  q0 = MFMA32(f1.a0, f1.x, q0);
  q1 = MFMA32(f1.a1, f1.x, q1);
  SBAR();
  dma_in_stage<I, 1>(c);
  if constexpr (I < 9) f0 = proj_load<I + 1, 0>(lds, xoff, woff0, woff1);
  SBAR();
  q0 = MFMA32(f2.a0, f2.x, q0);
  q1 = MFMA32(f2.a1, f2.x, q1);
  SBAR();
  dma_in_stage<I, 2>(c);
  if constexpr (I < 9) f1 = proj_load<I + 1, 1>(lds, xoff, woff0, woff1);
  SBAR();
  q0 = MFMA32(f3.a0, f3.x, q0);
  q1 = MFMA32(f3.a1, f3.x, q1);
  SBAR();
  dma_in_stage<I, 3>(c);
  if constexpr (I <= 7) wait_bar<proj_wait(I)>();
  else if constexpr (I == 8) lds_bar();
}

DI float max16(const f32x16& s) {
  float a = fmaxf(fmaxf(s[0], s[1]), s[2]), b = fmaxf(fmaxf(s[3], s[4]), s[5]);
  a = fmaxf(fmaxf(a, s[6]), s[7]);
  b = fmaxf(fmaxf(b, s[8]), s[9]);
  a = fmaxf(fmaxf(a, s[10]), s[11]);
  b = fmaxf(fmaxf(b, s[12]), s[13]);
  a = fmaxf(fmaxf(a, s[14]), s[15]);
  return fmaxf(a, b);
}
DI float swap32(float v) {
  auto r = __builtin_amdgcn_permlane32_swap(__float_as_uint(v), __float_as_uint(v), false, false);
  return (threadIdx.x & 32) ? __uint_as_float(r[0]) : __uint_as_float(r[1]);
}
DI float max_halves(float v) {
  auto r = __builtin_amdgcn_permlane32_swap(__float_as_uint(v), __float_as_uint(v), false, false);
  return fmaxf(__uint_as_float(r[0]), __uint_as_float(r[1]));
}
DI float sum_halves(float v) {
  auto r = __builtin_amdgcn_permlane32_swap(__float_as_uint(v), __float_as_uint(v), false, false);
  return __uint_as_float(r[0]) + __uint_as_float(r[1]);
}
constexpr float THR_LOG2 = 16.f;
constexpr float THR_RAW = THR_LOG2 / SCL;
struct SmState { float mref, l, fs, nmc; bool moved; };

DI void dma_wo(const K1Ctx& c, int gi) {
  const int pb = gi / 5, e = gi - 5 * pb;
  const unsigned ch = (unsigned)(64 * e + c.lane);
  const unsigned row = (ch * 205u) >> 11;
  glds16<true>(c.wob + (size_t)pb * 32 * 1280, row * 1280u + (ch - 10u * row) * 16u, c.lds0 + WO1 + 1024u * (unsigned)gi);
}
constexpr int wo_first(int s) { return s < 0 ? 0 : s > 10 ? 10 : s; }
constexpr int kv_extra(int s) { return s < 0 ? 0 : wo_first(s + 1) - wo_first(s); }
constexpr int kv_wait(int n) {
  int r = n == 0 ? 8 : 0;
  if (n > 0) for (int m = n + 1; m <= n + 3 && m <= 15; ++m) r += 2;
  for (int s = n - 4; s < n; ++s) r += kv_extra(s);
  return r;
}
template <int N>
DI void step_issue(const K1Ctx& c) {
  if constexpr (N >= 1 && N + 4 <= 15) dma_kv2<N + 4>(c);
#pragma unroll
  for (int j = wo_first(N); j < wo_first(N + 1); ++j) dma_wo(c, c.w + 8 * j);
}
template <int N>
DI void step_sync(const K1Ctx& c) {
  wait_bar<kv_wait(N)>();
}
template <bool FIRST>
DI void sm_decide(const f32x16& s, SmState& st) {
  const float tm = max_halves(max16(s));
  if constexpr (FIRST) {
    st.mref = tm;
    st.fs = 1.f;
    st.moved = false;
  } else {
    st.fs = 1.f;
    st.moved = false;
    if (__builtin_expect(__any(tm - st.mref > THR_RAW), 0)) {
      const float mn = fmaxf(st.mref, tm);
      st.fs = __builtin_amdgcn_exp2f((st.mref - mn) * SCL);
      st.mref = mn;
      st.moved = true;
    }
  }
  st.nmc = -st.mref * SCL;
}
DI unsigned sm_pair(const f32x16& s, int i, float nmc, float& la, float& lb) {
  const float e0 = __builtin_amdgcn_exp2f(__builtin_fmaf(s[2 * i], SCL, nmc));
  const float e1 = __builtin_amdgcn_exp2f(__builtin_fmaf(s[2 * i + 1], SCL, nmc));
  la += e0;
  lb += e1;
  asm volatile("" : "+v"(la), "+v"(lb));
  return cvtpk(e0, e1);
}
template <int MODE>
DI void sm_slot(int i, const f32x16& so, float nmc, float& la, float& lb, u32x4& w0, u32x4& w1) {
  if constexpr (MODE == 3) {
    const unsigned wd = sm_pair(so, i, nmc, la, lb);
    if (i < 4) w0[i] = wd; else w1[i - 4] = wd;
  } else if constexpr (MODE == 1) {
    if (i & 1) w0[i >> 1] = sm_pair(so, i >> 1, nmc, la, lb);
  } else if constexpr (MODE == 2) {
    if (i & 1) w1[i >> 1] = sm_pair(so, 4 + (i >> 1), nmc, la, lb);
  }
}
template <int MODE>
DI void sm_sum(SmState& st, float la, float lb) {
  if constexpr (MODE == 1 || MODE == 3) st.l = st.l * st.fs + (la + lb);
  else if constexpr (MODE == 2) st.l += la + lb;
}
template <int N, int MODE>
DI void k_step(const K1Ctx& c, lds_cptr lds, unsigned koff, const bf16x8 (&qf)[8], f32x16& sn, const f32x16& so,
               SmState& st, u32x4& w0, u32x4& w1) {
  step_sync<N>(c);
  unsigned kb = koff + kvaddr(N);
  asm volatile("" : "+v"(kb));
  bf16x8 kf[8];
#pragma unroll
  for (int ks = 0; ks < 8; ++ks) kf[ks] = ldsr128(lds + (kb ^ (unsigned)(ks * 32)));
  step_issue<N>(c);
  float la = 0.f, lb = 0.f;
  const float nmc = st.nmc;
  sn = f32x16{};
  SBAR();
#pragma unroll
  for (int ks = 0; ks < 8; ++ks) {
    sn = MFMA32(kf[ks], qf[ks], sn);
    sm_slot<MODE>(ks, so, nmc, la, lb, w0, w1);
    SBAR();
  }
  sm_sum<MODE>(st, la, lb);
}
template <int N, int MODE>
DI void v_step(const K1Ctx& c, lds_cptr lds, unsigned voff, const u32x4& p0, const u32x4& p1, float fs, bool moved,
               f32x16 (&o)[4], const f32x16& so, SmState& st, u32x4& w0, u32x4& w1) {
  step_sync<N>(c);
  unsigned vb = voff + kvaddr(N);
  asm volatile("" : "+v"(vb));
  bf16x8 vf[8];
#pragma unroll
  for (int dt = 0; dt < 4; ++dt) {
    lds_cptr vp = lds + (vb ^ (unsigned)(dt * 64));
    vf[2 * dt] = cat8(vtr(vp), vtr(vp + 2048));
    vf[2 * dt + 1] = cat8(vtr(vp + 4096), vtr(vp + 4096 + 2048));
  }
  step_issue<N>(c);
  if (__builtin_expect(moved, 0)) {
#pragma unroll
    for (int dt = 0; dt < 4; ++dt) o[dt] *= fs;
  }
  float la = 0.f, lb = 0.f;
  const float nmc = st.nmc;
  SBAR();
#pragma unroll
  for (int i = 0; i < 8; ++i) {
    o[i >> 1] = MFMA32(vf[i], __builtin_bit_cast(bf16x8, (i & 1) ? p1 : p0), o[i >> 1]);
    sm_slot<MODE>(i, so, nmc, la, lb, w0, w1);
    SBAR();
  }
  sm_sum<MODE>(st, la, lb);
}

DI unsigned ld_sc1(const char* p) { return __hip_atomic_load((const unsigned*)p, __ATOMIC_RELAXED, __HIP_MEMORY_SCOPE_AGENT); }
DI void st_sc1(char* p, unsigned v) { __hip_atomic_store((unsigned*)p, v, __ATOMIC_RELAXED, __HIP_MEMORY_SCOPE_AGENT); }

template <int LO, int NP>
DI void handoff(char* ws, int lane, unsigned my_off, unsigned peer_off, unsigned r, unsigned v) {
  const bool live = lane >= LO && lane < LO + NP;
  if (live) st_sc1(ws + WS_ACK + peer_off, v);
  for (int spin = 0; spin < (1 << 22); ++spin) {
    const unsigned a = ld_sc1(ws + WS_ACK + my_off);
    const unsigned vq = ld_sc1(ws + WS_REQ + my_off);
    if (live && vq != v) st_sc1(ws + WS_ACK + peer_off, vq);
    v = vq;
    if (__all(!live || a == r)) break;
    __builtin_amdgcn_s_sleep(1);
  }
  const unsigned vq = ld_sc1(ws + WS_REQ + my_off);
  if (live && vq != v) st_sc1(ws + WS_ACK + peer_off, vq);
}

__global__ __launch_bounds__(512, 2) void gqa_fused(const char* __restrict__ x, const char* __restrict__ kc,
                                                     const char* __restrict__ vc, const char* __restrict__ wq,
                                                     const char* __restrict__ wo, char* __restrict__ ws,
                                                     unsigned short* __restrict__ out) {
  extern __shared__ __attribute__((aligned(16))) char lds_raw[];
  const lds_ptr lds = (lds_ptr)lds_raw;
  const int tid = threadIdx.x, lane = tid & 63;
  const int w = __builtin_amdgcn_readfirstlane(tid >> 6);
  const int qt = w & 3, g = w >> 2;
  const int bid = blockIdx.x, xcd = bid & 7, idx = bid >> 3;
  const int h = (xcd & 3) * 8 + (idx & 7), b = (xcd >> 2) * 4 + (idx >> 3);
  const int k8 = h >> 2, bp = b >> 1, nt = (b & 1) * 4 + (h & 3);
  const int r32 = lane & 31, hh = lane >> 5, blk = (lane >> 4) & 1, q4 = (lane & 15) >> 2, p = lane & 3;

  const int sl = lane & 15;
  const unsigned my_off = (unsigned)((b * 32 + h) * 64 + 4 * sl);
  const unsigned peer_off = sl < 8 ? (unsigned)(((2 * bp + (sl >> 2)) * 32 + 4 * k8 + (sl & 3)) * 64 + 4 * nt)
                                   : (unsigned)((b * 32 + 4 * (sl - 8) + (h & 3)) * 64 + 32 + 4 * k8);
  unsigned rq = 0;
  if (w == 7) asm volatile("global_load_dword %0, %1, %2 sc1" : "=v"(rq) : "v"(my_off), "s"(ws + WS_ACK) : "memory");

  K1Ctx c;
  c.xb = x + (size_t)b * SQ * DM * 2;
  c.wqb = wq + h * 256;
  c.kb = kc + (size_t)(b * NH + h) * SK * DH * 2;
  c.vb = vc + (size_t)(b * NH + h) * SK * DH * 2;
  c.wob = wo + (size_t)k8 * 512 * 1280 + nt * 160;
  c.w = w;
  c.lane = lane;
  {
    const int xr0 = 16 * w + (lane >> 3), xr1 = xr0 + 8;
    c.vx0 = xr0 < SQ ? (unsigned)(xr0 * 1280 + (((lane & 7) ^ ((xr0 >> 1) & 7)) << 4)) : 0u;
    c.vx1 = xr1 < SQ ? (unsigned)(xr1 * 1280 + (((lane & 7) ^ ((xr1 >> 1) & 7)) << 4)) : 0u;
    const int r0 = 8 * w + (lane >> 4), r1 = r0 + 4;
    c.vw0 = (unsigned)(r0 * 8192 + (((lane & 15) ^ ((r0 & 3) << 2)) << 4));
    c.vw1 = (unsigned)(r1 * 8192 + (((lane & 15) ^ ((r1 & 3) << 2)) << 4));
    c.vk0 = (unsigned)(r0 * 256 + (((lane & 15) ^ (r0 & 15)) << 4));
    c.vk1 = (unsigned)(r1 * 256 + (((lane & 15) ^ (r1 & 15)) << 4));
    c.vv0 = (unsigned)(r0 * 256 + (((lane & 15) ^ ((r0 & 3) << 2)) << 4));
    c.vv1 = (unsigned)(r1 * 256 + (((lane & 15) ^ ((r1 & 3) << 2)) << 4));
  }
  c.lds0 = (unsigned)(uintptr_t)lds_raw;
  c.ldsw = c.lds0 + (unsigned)w * 2048u;
  c.rot = (((b & 3) * 5) / 2 + ((h & 7) * 5) / 4) % 10;

  dma_s<0, 0>(c); dma_s<0, 1>(c); dma_s<0, 2>(c); dma_s<0, 3>(c);
  dma_s<1, 0>(c); dma_s<1, 1>(c); dma_s<1, 2>(c); dma_s<1, 3>(c);
  dma_s<2, 0>(c); dma_s<2, 1>(c); dma_s<2, 2>(c); dma_s<2, 3>(c);

  f32x16 q0 = {}, q1 = {};
  {
    const int xrow = 32 * qt + r32;
    const unsigned xoff = (unsigned)(xrow * 128 + ((hh ^ ((xrow >> 1) & 7)) << 4));
    const int sp = ((p & 1) << 1) | (p >> 1);
    const int d0 = 2 * g, d1 = 2 * g + 1;
    const unsigned wrow = (unsigned)((8 * hh + q4) * 256 + 8 * (sp & 1));
    const unsigned woff0 = wrow + (unsigned)((4 * (d0 ^ q4) + 2 * blk + (sp >> 1)) << 4);
    const unsigned woff1 = wrow + (unsigned)((4 * (d1 ^ q4) + 2 * blk + (sp >> 1)) << 4);
    wait_bar<4>();
    asm volatile("" : "+v"(rq));
    rq += 1u;
    if (w == 7 && lane < 16) st_sc1(ws + WS_REQ + peer_off, rq);
    PFrag f0 = proj_load<0, 0>(lds, xoff, woff0, woff1);
    PFrag f1 = proj_load<0, 1>(lds, xoff, woff0, woff1);
    proj_stage<0>(c, lds, xoff, woff0, woff1, q0, q1, f0, f1);
    proj_stage<1>(c, lds, xoff, woff0, woff1, q0, q1, f0, f1);
    proj_stage<2>(c, lds, xoff, woff0, woff1, q0, q1, f0, f1);
    proj_stage<3>(c, lds, xoff, woff0, woff1, q0, q1, f0, f1);
    proj_stage<4>(c, lds, xoff, woff0, woff1, q0, q1, f0, f1);
    proj_stage<5>(c, lds, xoff, woff0, woff1, q0, q1, f0, f1);
    proj_stage<6>(c, lds, xoff, woff0, woff1, q0, q1, f0, f1);
    proj_stage<7>(c, lds, xoff, woff0, woff1, q0, q1, f0, f1);
    proj_stage<8>(c, lds, xoff, woff0, woff1, q0, q1, f0, f1);
    proj_stage<9>(c, lds, xoff, woff0, woff1, q0, q1, f0, f1);
  }
  bf16x8 qf[8];
  {
    lds_ptr e = lds + XSLOT * SLOT + qt * 8192 + g * 4096 + lane * 16;
    *(__attribute__((address_space(3))) bf16x8*)(e) = pack8<0>(q0);
    *(__attribute__((address_space(3))) bf16x8*)(e + 1024) = pack8<1>(q0);
    *(__attribute__((address_space(3))) bf16x8*)(e + 2048) = pack8<0>(q1);
    *(__attribute__((address_space(3))) bf16x8*)(e + 3072) = pack8<1>(q1);
  }
  lds_bar();
  dma_kv2<4>(c);
  {
    lds_cptr e = lds + XSLOT * SLOT + qt * 8192 + lane * 16;
#pragma unroll
    for (int ks = 0; ks < 8; ++ks) qf[ks] = ldsr128(e + ks * 1024);
  }
  f32x16 o[4] = {};
  SmState st;
  st.l = 0.f;
  float m, l;
  {
    const int krow = 32 * g + r32;
    const unsigned koff = (unsigned)(krow * 256 + ((hh ^ (krow & 15)) << 4));
    const unsigned voff = (unsigned)((32 * g + 4 * hh + q4) * 256 + 8 * (p & 1) + ((4 * q4 + 2 * blk + (p >> 1)) << 4));
    f32x16 sA, sB;
    u32x4 pa0, pa1, pb0, pb1;
    float fsp; bool mvp;
#define KSTEP(N, SN, SO, W0, W1)                                   \
    fsp = st.fs; mvp = st.moved;                                   \
    k_step<N, 2>(c, lds, koff, qf, SN, SO, st, W0, W1);            \
    sm_decide<false>(SN, st);
#define VSTEP(N, P0, P1, SO, W0, W1) v_step<N, 1>(c, lds, voff, P0, P1, fsp, mvp, o, SO, st, W0, W1);
    k_step<0, 0>(c, lds, koff, qf, sA, sA, st, pa0, pa1);
    sm_decide<true>(sA, st);
    fsp = 1.f; mvp = false;
    k_step<1, 3>(c, lds, koff, qf, sB, sA, st, pa0, pa1);
    sm_decide<false>(sB, st);
    VSTEP(2, pa0, pa1, sB, pb0, pb1)   KSTEP(3, sA, sB, pb0, pb1)
    VSTEP(4, pb0, pb1, sA, pa0, pa1)   KSTEP(5, sB, sA, pa0, pa1)
    VSTEP(6, pa0, pa1, sB, pb0, pb1)   KSTEP(7, sA, sB, pb0, pb1)
    VSTEP(8, pb0, pb1, sA, pa0, pa1)   KSTEP(9, sB, sA, pa0, pa1)
    VSTEP(10, pa0, pa1, sB, pb0, pb1)  KSTEP(11, sA, sB, pb0, pb1)
    VSTEP(12, pb0, pb1, sA, pa0, pa1)  KSTEP(13, sB, sA, pa0, pa1)
    {
      const float fs7 = st.fs; const bool mv7 = st.moved;
      v_step<14, 3>(c, lds, voff, pa0, pa1, fsp, mvp, o, sB, st, pb0, pb1);
      m = st.mref;
      l = sum_halves(st.l);
      *(__attribute__((address_space(3))) f32x2*)(lds + 2 * SLOT + w * 512 + lane * 8) = f32x2{m, l};
      v_step<15, 0>(c, lds, voff, pb0, pb1, fs7, mv7, o, sA, st, pa0, pa1);
    }
#undef KSTEP
#undef VSTEP
  }
  {
    const int w2 = w ^ 4;
    const f32x2 ml2 = *(const __attribute__((address_space(3))) f32x2*)(lds + 2 * SLOT + w2 * 512 + lane * 8);
    lds_bar();
    lds_ptr mb = lds + w * 8192 + lane * 16;
    f32x16 sa, sb, ka, kb2;
    if (g == 0) { sa = o[2]; sb = o[3]; ka = o[0]; kb2 = o[1]; }
    else        { sa = o[0]; sb = o[1]; ka = o[2]; kb2 = o[3]; }
#pragma unroll
    for (int i = 0; i < 4; ++i) {
      *(__attribute__((address_space(3))) f32x4*)(mb + i * 1024) = f32x4{sa[4 * i], sa[4 * i + 1], sa[4 * i + 2], sa[4 * i + 3]};
      *(__attribute__((address_space(3))) f32x4*)(mb + 4096 + i * 1024) = f32x4{sb[4 * i], sb[4 * i + 1], sb[4 * i + 2], sb[4 * i + 3]};
    }
    const float mt = fmaxf(m, ml2[0]);
    const float a1 = __builtin_amdgcn_exp2f((m - mt) * SCL), a2 = __builtin_amdgcn_exp2f((ml2[0] - mt) * SCL);
    const float inv = 1.0f / (a1 * l + a2 * ml2[1]);
    const float f1 = a1 * inv, f2 = a2 * inv;
    lds_bar();
    lds_cptr pb = lds + w2 * 8192 + lane * 16;
#pragma unroll
    for (int i = 0; i < 4; ++i) {
      const f32x4 u0 = *(const __attribute__((address_space(3))) f32x4*)(pb + i * 1024);
      const f32x4 u1 = *(const __attribute__((address_space(3))) f32x4*)(pb + 4096 + i * 1024);
#pragma unroll
      for (int j = 0; j < 4; ++j) {
        ka[4 * i + j] = ka[4 * i + j] * f1 + u0[j] * f2;
        kb2[4 * i + j] = kb2[4 * i + j] * f1 + u1[j] * f2;
      }
    }
    const int m16 = 2 * qt + (r32 >> 4);
    if (m16 < 7) {
      char* ob = ws + WS_O + ((size_t)((b * 128 + h * 4 + 2 * g) * 7 + m16)) * 1024 + (16 * hh + (r32 & 15)) * 16;
      auto pk = [](const f32x16& v, int u) {
        u32x4 t = {cvtpk(v[4 * u], v[4 * u + 1]), cvtpk(v[4 * u + 2], v[4 * u + 3]), cvtpk(v[4 * u + 8], v[4 * u + 9]),
                   cvtpk(v[4 * u + 10], v[4 * u + 11])};
        return __builtin_bit_cast(bf16x8, t);
      };
      store_wt(ob, pk(ka, 0));
      store_wt(ob + 512, pk(ka, 1));
      store_wt(ob + 7 * 1024, pk(kb2, 0));
      store_wt(ob + 7 * 1024 + 512, pk(kb2, 1));
    }
  }
  unsigned rv = 0;
  if (w == 7) rv = ld_sc1(ws + WS_REQ + my_off);
  wait_bar<0>();
  if (w == 7) handoff<0, 8>(ws, lane, my_off, peer_off, rq, rv);
  wait_bar<0>();
  const int gg = lane >> 4;
  const auto prs = __builtin_amdgcn_make_buffer_rsrc((void*)(ws + WS_PART + (size_t)(bp * 8 + nt) * 8 * 70 * 1024), 0,
                                                     8 * 70 * 1024, 0x00020000);
  f32x4 acc[2][5];
#pragma unroll
  for (int i = 0; i < 2; ++i)
#pragma unroll
    for (int j = 0; j < 5; ++j) acc[i][j] = f32x4{0.f, 0.f, 0.f, 0.f};
  if (w < 7) {
    const auto ors = __builtin_amdgcn_make_buffer_rsrc((void*)(ws + WS_O + (size_t)(2 * bp * 128 + 16 * k8) * 7 * 1024), 0,
                                                       (128 + 16) * 7 * 1024, 0x00020000);
    const int mo = w * 1024 + lane * 16;
    const unsigned wl = (unsigned)(WO1 + (4 * gg + q4) * 160 + 8 * p);
    constexpr int PD = 4;
    const int rot = 2 * nt;
    u32x4 of[PD][2];
#pragma unroll
    for (int ks = 0; ks < PD; ++ks) {
      of[ks][0] = __builtin_amdgcn_raw_buffer_load_b128(ors, mo, ((ks + rot) & 15) * 7168, 16);
      of[ks][1] = __builtin_amdgcn_raw_buffer_load_b128(ors, mo, (128 + ((ks + rot) & 15)) * 7168, 16);
    }
    SBAR();
#pragma unroll
    for (int ks = 0; ks < 16; ++ks) {
      lds_cptr wp = lds + (wl + (unsigned)(((ks + rot) & 15) * 5120));
      bf16x8 wf[5];
#pragma unroll
      for (int j = 0; j < 5; ++j) wf[j] = cat8(vtr(wp + 32 * j), vtr(wp + 32 * j + 2560));
      const bf16x8 o0 = __builtin_bit_cast(bf16x8, of[ks % PD][0]), o1 = __builtin_bit_cast(bf16x8, of[ks % PD][1]);
#pragma unroll
      for (int j = 0; j < 5; ++j) {
        acc[0][j] = MFMA16(wf[j], o0, acc[0][j]);
        acc[1][j] = MFMA16(wf[j], o1, acc[1][j]);
      }
      if (ks + PD < 16) {
        of[ks % PD][0] = __builtin_amdgcn_raw_buffer_load_b128(ors, mo, ((ks + PD + rot) & 15) * 7168, 16);
        of[ks % PD][1] = __builtin_amdgcn_raw_buffer_load_b128(ors, mo, (128 + ((ks + PD + rot) & 15)) * 7168, 16);
      }
      SBAR();
    }
#pragma unroll
    for (int i = 0; i < 2; ++i) {
      if (!(gg == (k8 & 3) && i == (k8 >> 2))) {
#pragma unroll
        for (int j = 0; j < 5; ++j)
          __builtin_amdgcn_raw_buffer_store_b128(__builtin_bit_cast(u32x4, acc[i][j]), prs,
                                                 (k8 * 70 + (i * 7 + w) * 5 + j) * 1024 + lane * 16, 0, 16);
      }
    }
  } else {
    rv = ld_sc1(ws + WS_REQ + my_off);
  }
  wait_bar<0>();
  if (w == 7) handoff<8, 8>(ws, lane, my_off, peer_off, rq, rv);
  wait_bar<0>();
  if (w < 7) {
    const int ih = k8 >> 2;
    const int po = ((ih * 7 + w) * 5) * 1024 + (16 * (k8 & 3) + (lane & 15)) * 16;
    u32x4 u[2][5];
#pragma unroll
    for (int j = 0; j < 5; ++j) {
      u[0][j] = __builtin_amdgcn_raw_buffer_load_b128(prs, gg * 70 * 1024 + po + j * 1024, 0, 16);
      u[1][j] = __builtin_amdgcn_raw_buffer_load_b128(prs, (gg + 4) * 70 * 1024 + po + j * 1024, 0, 16);
    }
    f32x4 r[5];
#pragma unroll
    for (int j = 0; j < 5; ++j) {
      const f32x4 own = ih ? acc[1][j] : acc[0][j];
      const f32x4 a = gg == k8 ? own : __builtin_bit_cast(f32x4, u[0][j]);
      const f32x4 bq = gg + 4 == k8 ? own : __builtin_bit_cast(f32x4, u[1][j]);
      f32x4 t = a + bq;
#pragma unroll
      for (int e = 0; e < 4; ++e) {
        t[e] += __shfl_xor(t[e], 16);
        t[e] += __shfl_xor(t[e], 32);
      }
      r[j] = t;
    }
    const f32x4 t0 = gg == 0 ? r[0] : gg == 1 ? r[1] : gg == 2 ? r[2] : r[3];
    unsigned short* op = out + (size_t)((2 * bp + ih) * SQ + 16 * w + (lane & 15)) * DM + 80 * nt + 4 * (k8 & 3);
    *(u32x2*)(op + 16 * gg) = u32x2{cvtpk(t0[0], t0[1]), cvtpk(t0[2], t0[3])};
    if (gg == 0) *(u32x2*)(op + 64) = u32x2{cvtpk(r[4][0], r[4][1]), cvtpk(r[4][2], r[4][3])};
  }
}

}

extern "C" void kernel_launch(void* const* d_in, const int* in_sizes, int n_in, void* d_out, int out_size, void* d_ws,
                              size_t ws_size, hipStream_t stream) {
  (void)in_sizes; (void)n_in; (void)out_size; (void)ws_size;
  (void)hipFuncSetAttribute((const void*)gqa_fused, hipFuncAttributeMaxDynamicSharedMemorySize, LDS1);
  hipLaunchKernelGGL(gqa_fused, dim3(256), dim3(512), LDS1, stream, (const char*)d_in[0], (const char*)d_in[1],
                     (const char*)d_in[2], (const char*)d_in[3], (const char*)d_in[4], (char*)d_ws,
                     (unsigned short*)d_out);
}
```

```cpp
#include <hip/hip_runtime.h>
#include <hip/hip_bf16.h>
#include <stdint.h>

namespace {

using bf16x8 = __attribute__((ext_vector_type(8))) short;
using s16x4 = __attribute__((ext_vector_type(4))) short;
using f32x16 = __attribute__((ext_vector_type(16))) float;
using f32x4 = __attribute__((ext_vector_type(4))) float;
using f32x2 = __attribute__((ext_vector_type(2))) float;
using u32x4 = __attribute__((ext_vector_type(4))) unsigned;
using u32x2 = __attribute__((ext_vector_type(2))) unsigned;
typedef short v4i16_t __attribute__((ext_vector_type(4)));
typedef __attribute__((address_space(3))) char* lds_ptr;
typedef __attribute__((address_space(3))) const char* lds_cptr;

#define DI __device__ __forceinline__
#define SBAR() __builtin_amdgcn_sched_barrier(0)
#define MFMA32(a, b, c) __builtin_amdgcn_mfma_f32_32x32x16_bf16((a), (b), (c), 0, 0, 0)
#define MFMA16(a, b, c) __builtin_amdgcn_mfma_f32_16x16x32_bf16((a), (b), (c), 0, 0, 0)

constexpr int NB = 8, SQ = 112, DM = 640, NH = 32, SK = 512, DH = 128;
constexpr int SLOT = 16384;
constexpr int LDS1 = 10 * SLOT;
constexpr float SCL = 0.08838834764831845f * 1.4426950408889634f;

constexpr size_t WS_REQ = 0;
constexpr size_t WS_ACK = 16384;
constexpr size_t WS_O = 32768;
constexpr size_t WS_O_BYTES = (size_t)NB * 128 * 7 * 1024;
constexpr size_t WS_PART = WS_O + WS_O_BYTES;
constexpr size_t WS_PART_BYTES = (size_t)32 * 8 * 14 * 2560;

DI s16x4 vtr(lds_cptr p) {
  return __builtin_bit_cast(s16x4, __builtin_amdgcn_ds_read_tr16_b64_v4i16((__attribute__((address_space(3))) v4i16_t*)p));
}
DI bf16x8 ldsr128(lds_cptr p) { return *(const __attribute__((address_space(3))) bf16x8*)p; }
DI bf16x8 cat8(s16x4 lo, s16x4 hi) { return __builtin_shufflevector(lo, hi, 0, 1, 2, 3, 4, 5, 6, 7); }
typedef __bf16 bfv2 __attribute__((ext_vector_type(2)));
DI unsigned cvtpk(float lo, float hi) {
  const f32x2 v = {lo, hi};
  return __builtin_bit_cast(unsigned, __builtin_convertvector(v, bfv2));
}
template <int S>
DI bf16x8 pack8(const f32x16& x) {
  u32x4 p = {cvtpk(x[8 * S + 0], x[8 * S + 1]), cvtpk(x[8 * S + 2], x[8 * S + 3]), cvtpk(x[8 * S + 4], x[8 * S + 5]),
             cvtpk(x[8 * S + 6], x[8 * S + 7])};
  return __builtin_bit_cast(bf16x8, p);
}
template <bool NT>
DI void glds16(const void* sbase, unsigned voff, unsigned lds_dst) {
  unsigned sv;
  if constexpr (NT)
    asm volatile("s_mov_b32 %0, m0\n\ts_mov_b32 m0, %3\n\ts_nop 2\n\tglobal_load_lds_dwordx4 %1, %2 nt\n\ts_mov_b32 m0, %0"
                 : "=&s"(sv) : "v"(voff), "s"(sbase), "s"(lds_dst) : "memory");
  else
    asm volatile("s_mov_b32 %0, m0\n\ts_mov_b32 m0, %3\n\ts_nop 2\n\tglobal_load_lds_dwordx4 %1, %2\n\ts_mov_b32 m0, %0"
                 : "=&s"(sv) : "v"(voff), "s"(sbase), "s"(lds_dst) : "memory");
}
template <int N>
DI void wait_bar() {
  asm volatile("s_waitcnt vmcnt(%0) lgkmcnt(0)\n\ts_barrier" ::"i"(N) : "memory");
}
DI void store_wt(char* p, bf16x8 v) {
  asm volatile("global_store_dwordx4 %0, %1, off sc1\n\ts_nop 1" ::"v"(p), "v"(v) : "memory");
}
DI void lds_bar() { asm volatile("s_waitcnt lgkmcnt(0)\n\ts_barrier" ::: "memory"); }

constexpr int sslot(int i) { return (2 * i) % 10; }
constexpr int kvaddr(int it) { return it % 5 < 4 ? (it % 5) * SLOT : 9 * SLOT; }
constexpr int XSLOT = 6;
constexpr int WO1 = 65536;

struct K1Ctx {
  const char* xb;
  const char* wqb;
  const char* kb;
  const char* vb;
  unsigned vx0, vx1, vw0, vw1, vk0, vk1, vv0, vv1;
  unsigned ldsw;
  int rot;
  const char* wob;
  unsigned lds0;
  int w, lane;
};

template <int I, int J>
DI void dma_s(const K1Ctx& c) {
  constexpr int sl = sslot(I);
  int st = I + c.rot;
  st = st >= 10 ? st - 10 : st;
  if constexpr (J == 0) glds16<false>(c.xb + st * 128, c.vx0, c.ldsw + sl * SLOT);
  if constexpr (J == 1) glds16<false>(c.wqb + (size_t)st * 64 * 8192, c.vw0, c.ldsw + (sl + 1) * SLOT);
  if constexpr (J == 2) glds16<false>(c.xb + st * 128, c.vx1, c.ldsw + sl * SLOT + 1024);
  if constexpr (J == 3) glds16<false>(c.wqb + (size_t)st * 64 * 8192, c.vw1, c.ldsw + (sl + 1) * SLOT + 1024);
}
constexpr bool pos_is_k(int n) { return n == 0 || ((n & 1) && n < 15); }
constexpr int pos_tile(int n) { return n == 0 ? 0 : (n == 15 ? 7 : ((n & 1) ? (n + 1) / 2 : n / 2 - 1)); }
template <int IT, int J>
DI void dma_kv(const K1Ctx& c) {
  if constexpr (pos_is_k(IT))
    glds16<true>(c.kb + pos_tile(IT) * 16384, J ? c.vk1 : c.vk0, c.ldsw + kvaddr(IT) + J * 1024);
  else
    glds16<true>(c.vb + pos_tile(IT) * 16384, J ? c.vv1 : c.vv0, c.ldsw + kvaddr(IT) + J * 1024);
}
template <int IT>
DI void dma_kv2(const K1Ctx& c) { dma_kv<IT, 0>(c); dma_kv<IT, 1>(c); }
constexpr int RA = 4;
constexpr int PDIST = 3;
template <int I, int J>
DI void dma_in_stage(const K1Ctx& c) {
  if constexpr (I + PDIST <= 9) dma_s<I + PDIST, J>(c);
  else if constexpr ((I - 7) * 2 + J / 2 < RA) dma_kv<(I - 7) * 2 + J / 2, J % 2>(c);
}
constexpr int proj_wait(int I) {
  int n = 0;
  for (int st = I + 3; st <= I + PDIST && st <= 9; ++st) n += 4;
  if (I == 7) n += 4;
  return n;
}
template <int N>
DI void wait_vm() { asm volatile("s_waitcnt vmcnt(%0)" ::"i"(N) : "memory"); }

struct PFrag { bf16x8 x, a0, a1; };
template <int I, int KS>
DI PFrag proj_load(lds_cptr lds, unsigned xoff, unsigned woff0, unsigned woff1) {
  lds_cptr xs = lds + sslot(I) * SLOT, ws = lds + (sslot(I) + 1) * SLOT;
  PFrag f;
  f.x = ldsr128(xs + (xoff ^ (unsigned)(KS * 32)));
  f.a0 = cat8(vtr(ws + woff0 + KS * 4096), vtr(ws + woff0 + KS * 4096 + 1024));
  f.a1 = cat8(vtr(ws + woff1 + KS * 4096), vtr(ws + woff1 + KS * 4096 + 1024));
  return f;
}
template <int I>
DI void proj_stage(const K1Ctx& c, lds_cptr lds, unsigned xoff, unsigned woff0, unsigned woff1, f32x16& q0, f32x16& q1,
                   PFrag& f0, PFrag& f1) {
  PFrag f2 = proj_load<I, 2>(lds, xoff, woff0, woff1);
  SBAR();
  q0 = MFMA32(f0.a0, f0.x, q0);
  q1 = MFMA32(f0.a1, f0.x, q1);
  SBAR();
  dma_in_stage<I, 0>(c);
  PFrag f3 = proj_load<I, 3>(lds, xoff, woff0, woff1);
  SBAR();
  q0 = MFMA32(f1.a0, f1.x, q0);
  q1 = MFMA32(f1.a1, f1.x, q1);
  SBAR();
  dma_in_stage<I, 1>(c);
  if constexpr (I < 9) f0 = proj_load<I + 1, 0>(lds, xoff, woff0, woff1);
  SBAR();
  q0 = MFMA32(f2.a0, f2.x, q0);
  q1 = MFMA32(f2.a1, f2.x, q1);
  SBAR();
  dma_in_stage<I, 2>(c);
  if constexpr (I < 9) f1 = proj_load<I + 1, 1>(lds, xoff, woff0, woff1);
  SBAR();
  q0 = MFMA32(f3.a0, f3.x, q0);
  q1 = MFMA32(f3.a1, f3.x, q1);
  SBAR();
  dma_in_stage<I, 3>(c);
  if constexpr (I <= 7) wait_bar<proj_wait(I)>();
  else if constexpr (I == 8) lds_bar();
}

DI float max16(const f32x16& s) {
  float a = fmaxf(fmaxf(s[0], s[1]), s[2]), b = fmaxf(fmaxf(s[3], s[4]), s[5]);
  a = fmaxf(fmaxf(a, s[6]), s[7]);
  b = fmaxf(fmaxf(b, s[8]), s[9]);
  a = fmaxf(fmaxf(a, s[10]), s[11]);
  b = fmaxf(fmaxf(b, s[12]), s[13]);
  a = fmaxf(fmaxf(a, s[14]), s[15]);
  return fmaxf(a, b);
}
DI float swap32(float v) {
  auto r = __builtin_amdgcn_permlane32_swap(__float_as_uint(v), __float_as_uint(v), false, false);
  return (threadIdx.x & 32) ? __uint_as_float(r[0]) : __uint_as_float(r[1]);
}
DI float max_halves(float v) {
  auto r = __builtin_amdgcn_permlane32_swap(__float_as_uint(v), __float_as_uint(v), false, false);
  return fmaxf(__uint_as_float(r[0]), __uint_as_float(r[1]));
}
DI float sum_halves(float v) {
  auto r = __builtin_amdgcn_permlane32_swap(__float_as_uint(v), __float_as_uint(v), false, false);
  return __uint_as_float(r[0]) + __uint_as_float(r[1]);
}
constexpr float THR_LOG2 = 16.f;
constexpr float THR_RAW = THR_LOG2 / SCL;
struct SmState { float mref, l, fs, nmc; bool moved; };

DI void dma_wo(const K1Ctx& c, int gi) {
  const int pb = gi / 5, e = gi - 5 * pb;
  const unsigned ch = (unsigned)(64 * e + c.lane);
  const unsigned row = (ch * 205u) >> 11;
  glds16<true>(c.wob + (size_t)pb * 32 * 1280, row * 1280u + (ch - 10u * row) * 16u, c.lds0 + WO1 + 1024u * (unsigned)gi);
}
constexpr int wo_first(int s) { return s < 0 ? 0 : s > 10 ? 10 : s; }
constexpr int kv_extra(int s) { return s < 0 ? 0 : wo_first(s + 1) - wo_first(s); }
constexpr int kv_wait(int n) {
  int r = n == 0 ? 8 : 0;
  if (n > 0) for (int m = n + 1; m <= n + 3 && m <= 15; ++m) r += 2;
  for (int s = n - 4; s < n; ++s) r += kv_extra(s);
  return r;
}
template <int N>
DI void step_issue(const K1Ctx& c) {
  if constexpr (N >= 1 && N + 4 <= 15) dma_kv2<N + 4>(c);
#pragma unroll
  for (int j = wo_first(N); j < wo_first(N + 1); ++j) dma_wo(c, c.w + 8 * j);
}
template <int N>
DI void step_sync(const K1Ctx& c) {
  wait_bar<kv_wait(N)>();
}
template <bool FIRST>
DI void sm_decide(const f32x16& s, SmState& st) {
  const float tm = max_halves(max16(s));
  if constexpr (FIRST) {
    st.mref = tm;
    st.fs = 1.f;
    st.moved = false;
  } else {
    st.fs = 1.f;
    st.moved = false;
    if (__builtin_expect(__any(tm - st.mref > THR_RAW), 0)) {
      const float mn = fmaxf(st.mref, tm);
      st.fs = __builtin_amdgcn_exp2f((st.mref - mn) * SCL);
      st.mref = mn;
      st.moved = true;
    }
  }
  st.nmc = -st.mref * SCL;
}
DI unsigned sm_pair(const f32x16& s, int i, float nmc, float& la, float& lb) {
  const float e0 = __builtin_amdgcn_exp2f(__builtin_fmaf(s[2 * i], SCL, nmc));
  const float e1 = __builtin_amdgcn_exp2f(__builtin_fmaf(s[2 * i + 1], SCL, nmc));
  la += e0;
  lb += e1;
  asm volatile("" : "+v"(la), "+v"(lb));
  return cvtpk(e0, e1);
}
template <int MODE>
DI void sm_slot(int i, const f32x16& so, float nmc, float& la, float& lb, u32x4& w0, u32x4& w1) {
  if constexpr (MODE == 3) {
    const unsigned wd = sm_pair(so, i, nmc, la, lb);
    if (i < 4) w0[i] = wd; else w1[i - 4] = wd;
  } else if constexpr (MODE == 1) {
    if (i & 1) w0[i >> 1] = sm_pair(so, i >> 1, nmc, la, lb);
  } else if constexpr (MODE == 2) {
    if (i & 1) w1[i >> 1] = sm_pair(so, 4 + (i >> 1), nmc, la, lb);
  }
}
template <int MODE>
DI void sm_sum(SmState& st, float la, float lb) {
  if constexpr (MODE == 1 || MODE == 3) st.l = st.l * st.fs + (la + lb);
  else if constexpr (MODE == 2) st.l += la + lb;
}
template <int N, int MODE>
DI void k_step(const K1Ctx& c, lds_cptr lds, unsigned koff, const bf16x8 (&qf)[8], f32x16& sn, const f32x16& so,
               SmState& st, u32x4& w0, u32x4& w1) {
  step_sync<N>(c);
  unsigned kb = koff + kvaddr(N);
  asm volatile("" : "+v"(kb));
  bf16x8 kf[8];
#pragma unroll
  for (int ks = 0; ks < 8; ++ks) kf[ks] = ldsr128(lds + (kb ^ (unsigned)(ks * 32)));
  step_issue<N>(c);
  float la = 0.f, lb = 0.f;
  const float nmc = st.nmc;
  sn = f32x16{};
  SBAR();
#pragma unroll
  for (int ks = 0; ks < 8; ++ks) {
    sn = MFMA32(kf[ks], qf[ks], sn);
    sm_slot<MODE>(ks, so, nmc, la, lb, w0, w1);
    SBAR();
  }
  sm_sum<MODE>(st, la, lb);
}
template <int N, int MODE>
DI void v_step(const K1Ctx& c, lds_cptr lds, unsigned voff, const u32x4& p0, const u32x4& p1, float fs, bool moved,
               f32x16 (&o)[4], const f32x16& so, SmState& st, u32x4& w0, u32x4& w1) {
  step_sync<N>(c);
  unsigned vb = voff + kvaddr(N);
  asm volatile("" : "+v"(vb));
  bf16x8 vf[8];
#pragma unroll
  for (int dt = 0; dt < 4; ++dt) {
    lds_cptr vp = lds + (vb ^ (unsigned)(dt * 64));
    vf[2 * dt] = cat8(vtr(vp), vtr(vp + 2048));
    vf[2 * dt + 1] = cat8(vtr(vp + 4096), vtr(vp + 4096 + 2048));
  }
  step_issue<N>(c);
  if (__builtin_expect(moved, 0)) {
#pragma unroll
    for (int dt = 0; dt < 4; ++dt) o[dt] *= fs;
  }
  float la = 0.f, lb = 0.f;
  const float nmc = st.nmc;
  SBAR();
#pragma unroll
  for (int i = 0; i < 8; ++i) {
    o[i >> 1] = MFMA32(vf[i], __builtin_bit_cast(bf16x8, (i & 1) ? p1 : p0), o[i >> 1]);
    sm_slot<MODE>(i, so, nmc, la, lb, w0, w1);
    SBAR();
  }
  sm_sum<MODE>(st, la, lb);
}

DI unsigned ld_sc1(const char* p) { return __hip_atomic_load((const unsigned*)p, __ATOMIC_RELAXED, __HIP_MEMORY_SCOPE_AGENT); }
DI void st_sc1(char* p, unsigned v) { __hip_atomic_store((unsigned*)p, v, __ATOMIC_RELAXED, __HIP_MEMORY_SCOPE_AGENT); }

template <int LO, int NP>
DI void handoff(char* ws, int lane, unsigned my_off, unsigned peer_off, unsigned r, unsigned v) {
  const bool live = lane >= LO && lane < LO + NP;
  if (live) st_sc1(ws + WS_ACK + peer_off, v);
  for (int spin = 0; spin < (1 << 22); ++spin) {
    const unsigned a = ld_sc1(ws + WS_ACK + my_off);
    const unsigned vq = ld_sc1(ws + WS_REQ + my_off);
    if (live && vq != v) st_sc1(ws + WS_ACK + peer_off, vq);
    v = vq;
    if (__all(!live || a == r)) break;
    __builtin_amdgcn_s_sleep(1);
  }
  const unsigned vq = ld_sc1(ws + WS_REQ + my_off);
  if (live && vq != v) st_sc1(ws + WS_ACK + peer_off, vq);
}

__global__ __launch_bounds__(512, 2) void gqa_fused(const char* __restrict__ x, const char* __restrict__ kc,
                                                     const char* __restrict__ vc, const char* __restrict__ wq,
                                                     const char* __restrict__ wo, char* __restrict__ ws,
                                                     unsigned short* __restrict__ out) {
  extern __shared__ __attribute__((aligned(16))) char lds_raw[];
  const lds_ptr lds = (lds_ptr)lds_raw;
  const int tid = threadIdx.x, lane = tid & 63;
  const int w = __builtin_amdgcn_readfirstlane(tid >> 6);
  const int qt = w & 3, g = w >> 2;
  const int bid = blockIdx.x, xcd = bid & 7, idx = bid >> 3;
  const int h = (xcd & 3) * 8 + (idx & 7), b = (xcd >> 2) * 4 + (idx >> 3);
  const int k8 = h >> 2, bp = b >> 1, nt = (b & 1) * 4 + (h & 3);
  const int r32 = lane & 31, hh = lane >> 5, blk = (lane >> 4) & 1, q4 = (lane & 15) >> 2, p = lane & 3;

  const int sl = lane & 15;
  const unsigned my_off = (unsigned)((b * 32 + h) * 64 + 4 * sl);
  const unsigned peer_off = sl < 8 ? (unsigned)(((2 * bp + (sl >> 2)) * 32 + 4 * k8 + (sl & 3)) * 64 + 4 * nt)
                                   : (unsigned)((b * 32 + 4 * (sl - 8) + (h & 3)) * 64 + 32 + 4 * k8);
  unsigned rq = 0;
  if (w == 7) asm volatile("global_load_dword %0, %1, %2 sc1" : "=v"(rq) : "v"(my_off), "s"(ws + WS_ACK) : "memory");

  K1Ctx c;
  c.xb = x + (size_t)b * SQ * DM * 2;
  c.wqb = wq + h * 256;
  c.kb = kc + (size_t)(b * NH + h) * SK * DH * 2;
  c.vb = vc + (size_t)(b * NH + h) * SK * DH * 2;
  c.wob = wo + (size_t)k8 * 512 * 1280 + nt * 160;
  c.w = w;
  c.lane = lane;
  {
    const int xr0 = 16 * w + (lane >> 3), xr1 = xr0 + 8;
    c.vx0 = xr0 < SQ ? (unsigned)(xr0 * 1280 + (((lane & 7) ^ ((xr0 >> 1) & 7)) << 4)) : 0u;
    c.vx1 = xr1 < SQ ? (unsigned)(xr1 * 1280 + (((lane & 7) ^ ((xr1 >> 1) & 7)) << 4)) : 0u;
    const int r0 = 8 * w + (lane >> 4), r1 = r0 + 4;
    c.vw0 = (unsigned)(r0 * 8192 + (((lane & 15) ^ ((r0 & 3) << 2)) << 4));
    c.vw1 = (unsigned)(r1 * 8192 + (((lane & 15) ^ ((r1 & 3) << 2)) << 4));
    c.vk0 = (unsigned)(r0 * 256 + (((lane & 15) ^ (r0 & 15)) << 4));
    c.vk1 = (unsigned)(r1 * 256 + (((lane & 15) ^ (r1 & 15)) << 4));
    c.vv0 = (unsigned)(r0 * 256 + (((lane & 15) ^ ((r0 & 3) << 2)) << 4));
    c.vv1 = (unsigned)(r1 * 256 + (((lane & 15) ^ ((r1 & 3) << 2)) << 4));
  }
  c.lds0 = (unsigned)(uintptr_t)lds_raw;
  c.ldsw = c.lds0 + (unsigned)w * 2048u;
  c.rot = (((b & 3) * 5) / 2 + ((h & 7) * 5) / 4) % 10;

  dma_s<0, 0>(c); dma_s<0, 1>(c); dma_s<0, 2>(c); dma_s<0, 3>(c);
  dma_s<1, 0>(c); dma_s<1, 1>(c); dma_s<1, 2>(c); dma_s<1, 3>(c);
  dma_s<2, 0>(c); dma_s<2, 1>(c); dma_s<2, 2>(c); dma_s<2, 3>(c);

  f32x16 q0 = {}, q1 = {};
  {
    const int xrow = 32 * qt + r32;
    const unsigned xoff = (unsigned)(xrow * 128 + ((hh ^ ((xrow >> 1) & 7)) << 4));
    const int sp = ((p & 1) << 1) | (p >> 1);
    const int d0 = 2 * g, d1 = 2 * g + 1;
    const unsigned wrow = (unsigned)((8 * hh + q4) * 256 + 8 * (sp & 1));
    const unsigned woff0 = wrow + (unsigned)((4 * (d0 ^ q4) + 2 * blk + (sp >> 1)) << 4);
    const unsigned woff1 = wrow + (unsigned)((4 * (d1 ^ q4) + 2 * blk + (sp >> 1)) << 4);
    wait_bar<4>();
    asm volatile("" : "+v"(rq));
    rq += 1u;
    if (w == 7 && lane < 16) st_sc1(ws + WS_REQ + peer_off, rq);
    PFrag f0 = proj_load<0, 0>(lds, xoff, woff0, woff1);
    PFrag f1 = proj_load<0, 1>(lds, xoff, woff0, woff1);
    proj_stage<0>(c, lds, xoff, woff0, woff1, q0, q1, f0, f1);
    proj_stage<1>(c, lds, xoff, woff0, woff1, q0, q1, f0, f1);
    proj_stage<2>(c, lds, xoff, woff0, woff1, q0, q1, f0, f1);
    proj_stage<3>(c, lds, xoff, woff0, woff1, q0, q1, f0, f1);
    proj_stage<4>(c, lds, xoff, woff0, woff1, q0, q1, f0, f1);
    proj_stage<5>(c, lds, xoff, woff0, woff1, q0, q1, f0, f1);
    proj_stage<6>(c, lds, xoff, woff0, woff1, q0, q1, f0, f1);
    proj_stage<7>(c, lds, xoff, woff0, woff1, q0, q1, f0, f1);
    proj_stage<8>(c, lds, xoff, woff0, woff1, q0, q1, f0, f1);
    proj_stage<9>(c, lds, xoff, woff0, woff1, q0, q1, f0, f1);
  }
  bf16x8 qf[8];
  {
    lds_ptr e = lds + XSLOT * SLOT + qt * 8192 + g * 4096 + lane * 16;
    *(__attribute__((address_space(3))) bf16x8*)(e) = pack8<0>(q0);
    *(__attribute__((address_space(3))) bf16x8*)(e + 1024) = pack8<1>(q0);
    *(__attribute__((address_space(3))) bf16x8*)(e + 2048) = pack8<0>(q1);
    *(__attribute__((address_space(3))) bf16x8*)(e + 3072) = pack8<1>(q1);
  }
  lds_bar();
  dma_kv2<4>(c);
  {
    lds_cptr e = lds + XSLOT * SLOT + qt * 8192 + lane * 16;
#pragma unroll
    for (int ks = 0; ks < 8; ++ks) qf[ks] = ldsr128(e + ks * 1024);
  }
  f32x16 o[4] = {};
  SmState st;
  st.l = 0.f;
  float m, l;
  {
    const int krow = 32 * g + r32;
    const unsigned koff = (unsigned)(krow * 256 + ((hh ^ (krow & 15)) << 4));
    const unsigned voff = (unsigned)((32 * g + 4 * hh + q4) * 256 + 8 * (p & 1) + ((4 * q4 + 2 * blk + (p >> 1)) << 4));
    f32x16 sA, sB;
    u32x4 pa0, pa1, pb0, pb1;
    float fsp; bool mvp;
#define KSTEP(N, SN, SO, W0, W1)                                   \
    fsp = st.fs; mvp = st.moved;                                   \
    k_step<N, 2>(c, lds, koff, qf, SN, SO, st, W0, W1);            \
    sm_decide<false>(SN, st);
#define VSTEP(N, P0, P1, SO, W0, W1) v_step<N, 1>(c, lds, voff, P0, P1, fsp, mvp, o, SO, st, W0, W1);
    k_step<0, 0>(c, lds, koff, qf, sA, sA, st, pa0, pa1);
    sm_decide<true>(sA, st);
    fsp = 1.f; mvp = false;
    k_step<1, 3>(c, lds, koff, qf, sB, sA, st, pa0, pa1);
    sm_decide<false>(sB, st);
    VSTEP(2, pa0, pa1, sB, pb0, pb1)   KSTEP(3, sA, sB, pb0, pb1)
    VSTEP(4, pb0, pb1, sA, pa0, pa1)   KSTEP(5, sB, sA, pa0, pa1)
    VSTEP(6, pa0, pa1, sB, pb0, pb1)   KSTEP(7, sA, sB, pb0, pb1)
    VSTEP(8, pb0, pb1, sA, pa0, pa1)   KSTEP(9, sB, sA, pa0, pa1)
    VSTEP(10, pa0, pa1, sB, pb0, pb1)  KSTEP(11, sA, sB, pb0, pb1)
    VSTEP(12, pb0, pb1, sA, pa0, pa1)  KSTEP(13, sB, sA, pa0, pa1)
    {
      const float fs7 = st.fs; const bool mv7 = st.moved;
      v_step<14, 3>(c, lds, voff, pa0, pa1, fsp, mvp, o, sB, st, pb0, pb1);
      m = st.mref;
      l = sum_halves(st.l);
      *(__attribute__((address_space(3))) f32x2*)(lds + 2 * SLOT + w * 512 + lane * 8) = f32x2{m, l};
      v_step<15, 0>(c, lds, voff, pb0, pb1, fs7, mv7, o, sA, st, pa0, pa1);
    }
#undef KSTEP
#undef VSTEP
  }
  {
    const int w2 = w ^ 4;
    const f32x2 ml2 = *(const __attribute__((address_space(3))) f32x2*)(lds + 2 * SLOT + w2 * 512 + lane * 8);
    lds_bar();
    lds_ptr mb = lds + w * 8192 + lane * 16;
    f32x16 sa, sb, ka, kb2;
    if (g == 0) { sa = o[2]; sb = o[3]; ka = o[0]; kb2 = o[1]; }
    else        { sa = o[0]; sb = o[1]; ka = o[2]; kb2 = o[3]; }
#pragma unroll
    for (int i = 0; i < 4; ++i) {
      *(__attribute__((address_space(3))) f32x4*)(mb + i * 1024) = f32x4{sa[4 * i], sa[4 * i + 1], sa[4 * i + 2], sa[4 * i + 3]};
      *(__attribute__((address_space(3))) f32x4*)(mb + 4096 + i * 1024) = f32x4{sb[4 * i], sb[4 * i + 1], sb[4 * i + 2], sb[4 * i + 3]};
    }
    const float mt = fmaxf(m, ml2[0]);
    const float a1 = __builtin_amdgcn_exp2f((m - mt) * SCL), a2 = __builtin_amdgcn_exp2f((ml2[0] - mt) * SCL);
    const float inv = 1.0f / (a1 * l + a2 * ml2[1]);
    const float f1 = a1 * inv, f2 = a2 * inv;
    lds_bar();
    lds_cptr pb = lds + w2 * 8192 + lane * 16;
#pragma unroll
    for (int i = 0; i < 4; ++i) {
      const f32x4 u0 = *(const __attribute__((address_space(3))) f32x4*)(pb + i * 1024);
      const f32x4 u1 = *(const __attribute__((address_space(3))) f32x4*)(pb + 4096 + i * 1024);
#pragma unroll
      for (int j = 0; j < 4; ++j) {
        ka[4 * i + j] = ka[4 * i + j] * f1 + u0[j] * f2;
        kb2[4 * i + j] = kb2[4 * i + j] * f1 + u1[j] * f2;
      }
    }
    const int m16 = 2 * qt + (r32 >> 4);
    if (m16 < 7) {
      char* ob = ws + WS_O + ((size_t)((b * 128 + h * 4 + 2 * g) * 7 + m16)) * 1024 + (16 * hh + (r32 & 15)) * 16;
      auto pk = [](const f32x16& v, int u) {
        u32x4 t = {cvtpk(v[4 * u], v[4 * u + 1]), cvtpk(v[4 * u + 2], v[4 * u + 3]), cvtpk(v[4 * u + 8], v[4 * u + 9]),
                   cvtpk(v[4 * u + 10], v[4 * u + 11])};
        return __builtin_bit_cast(bf16x8, t);
      };
      store_wt(ob, pk(ka, 0));
      store_wt(ob + 512, pk(ka, 1));
      store_wt(ob + 7 * 1024, pk(kb2, 0));
      store_wt(ob + 7 * 1024 + 512, pk(kb2, 1));
    }
  }
  unsigned rv = 0;
  if (w == 7) rv = ld_sc1(ws + WS_REQ + my_off);
  wait_bar<0>();
  if (w == 7) handoff<0, 8>(ws, lane, my_off, peer_off, rq, rv);
  wait_bar<0>();
  const int gg = lane >> 4;
  const auto prs = __builtin_amdgcn_make_buffer_rsrc((void*)(ws + WS_PART + (size_t)(bp * 8 + nt) * 8 * 14 * 2560), 0,
                                                     8 * 14 * 2560, 0x00020000);
  f32x4 acc[2][5];
#pragma unroll
  for (int i = 0; i < 2; ++i)
#pragma unroll
    for (int j = 0; j < 5; ++j) acc[i][j] = f32x4{0.f, 0.f, 0.f, 0.f};
  if (w < 7) {
    const auto ors = __builtin_amdgcn_make_buffer_rsrc((void*)(ws + WS_O + (size_t)(2 * bp * 128 + 16 * k8) * 7 * 1024), 0,
                                                       (128 + 16) * 7 * 1024, 0x00020000);
    const int mo = w * 1024 + lane * 16;
    const unsigned wl = (unsigned)(WO1 + (4 * gg + q4) * 160 + 8 * p);
    constexpr int PD = 4;
    const int rot = 2 * nt;
    u32x4 of[PD][2];
#pragma unroll
    for (int ks = 0; ks < PD; ++ks) {
      of[ks][0] = __builtin_amdgcn_raw_buffer_load_b128(ors, mo, ((ks + rot) & 15) * 7168, 16);
      of[ks][1] = __builtin_amdgcn_raw_buffer_load_b128(ors, mo, (128 + ((ks + rot) & 15)) * 7168, 16);
    }
    SBAR();
#pragma unroll
    for (int ks = 0; ks < 16; ++ks) {
      lds_cptr wp = lds + (wl + (unsigned)(((ks + rot) & 15) * 5120));
      bf16x8 wf[5];
#pragma unroll
      for (int j = 0; j < 5; ++j) wf[j] = cat8(vtr(wp + 32 * j), vtr(wp + 32 * j + 2560));
      const bf16x8 o0 = __builtin_bit_cast(bf16x8, of[ks % PD][0]), o1 = __builtin_bit_cast(bf16x8, of[ks % PD][1]);
#pragma unroll
      for (int j = 0; j < 5; ++j) {
        acc[0][j] = MFMA16(wf[j], o0, acc[0][j]);
        acc[1][j] = MFMA16(wf[j], o1, acc[1][j]);
      }
      if (ks + PD < 16) {
        of[ks % PD][0] = __builtin_amdgcn_raw_buffer_load_b128(ors, mo, ((ks + PD + rot) & 15) * 7168, 16);
        of[ks % PD][1] = __builtin_amdgcn_raw_buffer_load_b128(ors, mo, (128 + ((ks + PD + rot) & 15)) * 7168, 16);
      }
      SBAR();
    }
#pragma unroll
    for (int i = 0; i < 2; ++i) {
      if (!(gg == (k8 & 3) && i == (k8 >> 2))) {
        const int po = (k8 * 14 + i * 7 + w) * 2560;
        const u32x4 ra = {cvtpk(acc[i][0][0], acc[i][0][1]), cvtpk(acc[i][0][2], acc[i][0][3]),
                          cvtpk(acc[i][1][0], acc[i][1][1]), cvtpk(acc[i][1][2], acc[i][1][3])};
        const u32x4 rb = {cvtpk(acc[i][2][0], acc[i][2][1]), cvtpk(acc[i][2][2], acc[i][2][3]),
                          cvtpk(acc[i][3][0], acc[i][3][1]), cvtpk(acc[i][3][2], acc[i][3][3])};
        const u32x2 rc = {cvtpk(acc[i][4][0], acc[i][4][1]), cvtpk(acc[i][4][2], acc[i][4][3])};
        __builtin_amdgcn_raw_buffer_store_b128(ra, prs, po + lane * 16, 0, 16);
        __builtin_amdgcn_raw_buffer_store_b128(rb, prs, po + 1024 + lane * 16, 0, 16);
        __builtin_amdgcn_raw_buffer_store_b64(rc, prs, po + 2048 + lane * 8, 0, 16);
      }
    }
  } else {
    rv = ld_sc1(ws + WS_REQ + my_off);
  }
  wait_bar<0>();
  if (w == 7) handoff<8, 8>(ws, lane, my_off, peer_off, rq, rv);
  wait_bar<0>();
  if (w < 7) {
    const int ih = k8 >> 2;
    const int sl2 = 16 * (k8 & 3) + (lane & 15);
    u32x4 ua[2], ub[2];
    u32x2 uc[2];
#pragma unroll
    for (int q = 0; q < 2; ++q) {
      const int po = ((gg + 4 * q) * 14 + ih * 7 + w) * 2560;
      ua[q] = __builtin_amdgcn_raw_buffer_load_b128(prs, po + sl2 * 16, 0, 16);
      ub[q] = __builtin_amdgcn_raw_buffer_load_b128(prs, po + 1024 + sl2 * 16, 0, 16);
      uc[q] = __builtin_amdgcn_raw_buffer_load_b64(prs, po + 2048 + sl2 * 8, 0, 16);
    }
    auto unpk = [](unsigned lo, unsigned hi) {
      return f32x4{__uint_as_float(lo << 16), __uint_as_float(lo & 0xffff0000u), __uint_as_float(hi << 16),
                   __uint_as_float(hi & 0xffff0000u)};
    };
    f32x4 r[5];
#pragma unroll
    for (int j = 0; j < 5; ++j) {
      const f32x4 own = ih ? acc[1][j] : acc[0][j];
      f32x4 v2[2];
#pragma unroll
      for (int q = 0; q < 2; ++q) {
        const f32x4 pv = j == 0 ? unpk(ua[q][0], ua[q][1]) : j == 1 ? unpk(ua[q][2], ua[q][3])
                       : j == 2 ? unpk(ub[q][0], ub[q][1]) : j == 3 ? unpk(ub[q][2], ub[q][3]) : unpk(uc[q][0], uc[q][1]);
        v2[q] = gg + 4 * q == k8 ? own : pv;
      }
      f32x4 t = v2[0] + v2[1];
#pragma unroll
      for (int e = 0; e < 4; ++e) {
        t[e] += __shfl_xor(t[e], 16);
        t[e] += __shfl_xor(t[e], 32);
      }
      r[j] = t;
    }
    const f32x4 t0 = gg == 0 ? r[0] : gg == 1 ? r[1] : gg == 2 ? r[2] : r[3];
    unsigned short* op = out + (size_t)((2 * bp + ih) * SQ + 16 * w + (lane & 15)) * DM + 80 * nt + 4 * (k8 & 3);
    *(u32x2*)(op + 16 * gg) = u32x2{cvtpk(t0[0], t0[1]), cvtpk(t0[2], t0[3])};
    if (gg == 0) *(u32x2*)(op + 64) = u32x2{cvtpk(r[4][0], r[4][1]), cvtpk(r[4][2], r[4][3])};
  }
}

}

extern "C" void kernel_launch(void* const* d_in, const int* in_sizes, int n_in, void* d_out, int out_size, void* d_ws,
                              size_t ws_size, hipStream_t stream) {
  (void)in_sizes; (void)n_in; (void)out_size; (void)ws_size;
  (void)hipFuncSetAttribute((const void*)gqa_fused, hipFuncAttributeMaxDynamicSharedMemorySize, LDS1);
  hipLaunchKernelGGL(gqa_fused, dim3(256), dim3(512), LDS1, stream, (const char*)d_in[0], (const char*)d_in[1],
                     (const char*)d_in[2], (const char*)d_in[3], (const char*)d_in[4], (char*)d_ws,
                     (unsigned short*)d_out);
}
```

```cpp
#include <hip/hip_runtime.h>
#include <hip/hip_bf16.h>
#include <stdint.h>

namespace {

using bf16x8 = __attribute__((ext_vector_type(8))) short;
using s16x4 = __attribute__((ext_vector_type(4))) short;
using f32x16 = __attribute__((ext_vector_type(16))) float;
using f32x4 = __attribute__((ext_vector_type(4))) float;
using f32x2 = __attribute__((ext_vector_type(2))) float;
using u32x4 = __attribute__((ext_vector_type(4))) unsigned;
using u32x2 = __attribute__((ext_vector_type(2))) unsigned;
typedef short v4i16_t __attribute__((ext_vector_type(4)));
typedef __attribute__((address_space(3))) char* lds_ptr;
typedef __attribute__((address_space(3))) const char* lds_cptr;

#define DI __device__ __forceinline__
#define SBAR() __builtin_amdgcn_sched_barrier(0)
#define MFMA32(a, b, c) __builtin_amdgcn_mfma_f32_32x32x16_bf16((a), (b), (c), 0, 0, 0)
#define MFMA16(a, b, c) __builtin_amdgcn_mfma_f32_16x16x32_bf16((a), (b), (c), 0, 0, 0)

constexpr int NB = 8, SQ = 112, DM = 640, NH = 32, SK = 512, DH = 128;
constexpr int SLOT = 16384;
constexpr int LDS1 = 10 * SLOT;
constexpr float SCL = 0.08838834764831845f * 1.4426950408889634f;

constexpr size_t WS_REQ = 0;
constexpr size_t WS_ACK = 16384;
constexpr size_t WS_O = 32768;
constexpr size_t WS_O_BYTES = (size_t)NB * 128 * 7 * 1024;
constexpr size_t WS_PART = WS_O + WS_O_BYTES;
constexpr size_t WS_PART_BYTES = (size_t)32 * 8 * 14 * 2560;

DI s16x4 vtr(lds_cptr p) {
  return __builtin_bit_cast(s16x4, __builtin_amdgcn_ds_read_tr16_b64_v4i16((__attribute__((address_space(3))) v4i16_t*)p));
}
DI bf16x8 ldsr128(lds_cptr p) { return *(const __attribute__((address_space(3))) bf16x8*)p; }
DI bf16x8 cat8(s16x4 lo, s16x4 hi) { return __builtin_shufflevector(lo, hi, 0, 1, 2, 3, 4, 5, 6, 7); }
typedef __bf16 bfv2 __attribute__((ext_vector_type(2)));
DI unsigned cvtpk(float lo, float hi) {
  const f32x2 v = {lo, hi};
  return __builtin_bit_cast(unsigned, __builtin_convertvector(v, bfv2));
}
template <int S>
DI bf16x8 pack8(const f32x16& x) {
  u32x4 p = {cvtpk(x[8 * S + 0], x[8 * S + 1]), cvtpk(x[8 * S + 2], x[8 * S + 3]), cvtpk(x[8 * S + 4], x[8 * S + 5]),
             cvtpk(x[8 * S + 6], x[8 * S + 7])};
  return __builtin_bit_cast(bf16x8, p);
}
template <bool NT>
DI void glds16(const void* sbase, unsigned voff, unsigned lds_dst) {
  unsigned sv;
  if constexpr (NT)
    asm volatile("s_mov_b32 %0, m0\n\ts_mov_b32 m0, %3\n\ts_nop 2\n\tglobal_load_lds_dwordx4 %1, %2 nt\n\ts_mov_b32 m0, %0"
                 : "=&s"(sv) : "v"(voff), "s"(sbase), "s"(lds_dst) : "memory");
  else
    asm volatile("s_mov_b32 %0, m0\n\ts_mov_b32 m0, %3\n\ts_nop 2\n\tglobal_load_lds_dwordx4 %1, %2\n\ts_mov_b32 m0, %0"
                 : "=&s"(sv) : "v"(voff), "s"(sbase), "s"(lds_dst) : "memory");
}
template <int N>
DI void wait_bar() {
  asm volatile("s_waitcnt vmcnt(%0) lgkmcnt(0)\n\ts_barrier" ::"i"(N) : "memory");
}
DI void store_wt(char* p, bf16x8 v) {
  asm volatile("global_store_dwordx4 %0, %1, off sc1\n\ts_nop 1" ::"v"(p), "v"(v) : "memory");
}
DI void lds_bar() { asm volatile("s_waitcnt lgkmcnt(0)\n\ts_barrier" ::: "memory"); }

constexpr int sslot(int i) { return (2 * i) % 10; }
constexpr int kvaddr(int it) { return it % 5 < 4 ? (it % 5) * SLOT : 9 * SLOT; }
constexpr int XSLOT = 6;
constexpr int WO1 = 65536;

struct K1Ctx {
  const char* xb;
  const char* wqb;
  const char* kb;
  const char* vb;
  unsigned vx0, vx1, vw0, vw1, vk0, vk1, vv0, vv1;
  unsigned ldsw;
  int rot;
  const char* wob;
  unsigned lds0;
  int w, lane;
};

template <int I, int J>
DI void dma_s(const K1Ctx& c) {
  constexpr int sl = sslot(I);
  int st = I + c.rot;
  st = st >= 10 ? st - 10 : st;
  if constexpr (J == 0) glds16<false>(c.xb + st * 128, c.vx0, c.ldsw + sl * SLOT);
  if constexpr (J == 1) glds16<false>(c.wqb + (size_t)st * 64 * 8192, c.vw0, c.ldsw + (sl + 1) * SLOT);
  if constexpr (J == 2) glds16<false>(c.xb + st * 128, c.vx1, c.ldsw + sl * SLOT + 1024);
  if constexpr (J == 3) glds16<false>(c.wqb + (size_t)st * 64 * 8192, c.vw1, c.ldsw + (sl + 1) * SLOT + 1024);
}
constexpr bool pos_is_k(int n) { return n == 0 || ((n & 1) && n < 15); }
constexpr int pos_tile(int n) { return n == 0 ? 0 : (n == 15 ? 7 : ((n & 1) ? (n + 1) / 2 : n / 2 - 1)); }
template <int IT, int J>
DI void dma_kv(const K1Ctx& c) {
  if constexpr (pos_is_k(IT))
    glds16<true>(c.kb + pos_tile(IT) * 16384, J ? c.vk1 : c.vk0, c.ldsw + kvaddr(IT) + J * 1024);
  else
    glds16<true>(c.vb + pos_tile(IT) * 16384, J ? c.vv1 : c.vv0, c.ldsw + kvaddr(IT) + J * 1024);
}
template <int IT>
DI void dma_kv2(const K1Ctx& c) { dma_kv<IT, 0>(c); dma_kv<IT, 1>(c); }
constexpr int RA = 4;
constexpr int PDIST = 3;
template <int I, int J>
DI void dma_in_stage(const K1Ctx& c) {
  if constexpr (I + PDIST <= 9) dma_s<I + PDIST, J>(c);
  else if constexpr ((I - 7) * 2 + J / 2 < RA) dma_kv<(I - 7) * 2 + J / 2, J % 2>(c);
}
constexpr int proj_wait(int I) {
  int n = 0;
  for (int st = I + 3; st <= I + PDIST && st <= 9; ++st) n += 4;
  if (I == 7) n += 4;
  return n;
}

struct PFrag { bf16x8 x, a0, a1; };
template <int I, int KS>
DI PFrag proj_load(lds_cptr lds, unsigned xoff, unsigned woff0, unsigned woff1) {
  lds_cptr xs = lds + sslot(I) * SLOT, ws = lds + (sslot(I) + 1) * SLOT;
  PFrag f;
  f.x = ldsr128(xs + (xoff ^ (unsigned)(KS * 32)));
  f.a0 = cat8(vtr(ws + woff0 + KS * 4096), vtr(ws + woff0 + KS * 4096 + 1024));
  f.a1 = cat8(vtr(ws + woff1 + KS * 4096), vtr(ws + woff1 + KS * 4096 + 1024));
  return f;
}
template <int I>
DI void proj_stage(const K1Ctx& c, lds_cptr lds, unsigned xoff, unsigned woff0, unsigned woff1, f32x16& q0, f32x16& q1,
                   PFrag& f0, PFrag& f1) {
  PFrag f2 = proj_load<I, 2>(lds, xoff, woff0, woff1);
  SBAR();
  q0 = MFMA32(f0.a0, f0.x, q0);
  q1 = MFMA32(f0.a1, f0.x, q1);
  SBAR();
  dma_in_stage<I, 0>(c);
  PFrag f3 = proj_load<I, 3>(lds, xoff, woff0, woff1);
  SBAR();
  q0 = MFMA32(f1.a0, f1.x, q0);
  q1 = MFMA32(f1.a1, f1.x, q1);
  SBAR();
  dma_in_stage<I, 1>(c);
  if constexpr (I < 9) f0 = proj_load<I + 1, 0>(lds, xoff, woff0, woff1);
  SBAR();
  q0 = MFMA32(f2.a0, f2.x, q0);
  q1 = MFMA32(f2.a1, f2.x, q1);
  SBAR();
  dma_in_stage<I, 2>(c);
  if constexpr (I < 9) f1 = proj_load<I + 1, 1>(lds, xoff, woff0, woff1);
  SBAR();
  q0 = MFMA32(f3.a0, f3.x, q0);
  q1 = MFMA32(f3.a1, f3.x, q1);
  SBAR();
  dma_in_stage<I, 3>(c);
  if constexpr (I <= 7) wait_bar<proj_wait(I)>();
  else if constexpr (I == 8) lds_bar();
}

DI float max16(const f32x16& s) {
  float a = fmaxf(fmaxf(s[0], s[1]), s[2]), b = fmaxf(fmaxf(s[3], s[4]), s[5]);
  a = fmaxf(fmaxf(a, s[6]), s[7]);
  b = fmaxf(fmaxf(b, s[8]), s[9]);
  a = fmaxf(fmaxf(a, s[10]), s[11]);
  b = fmaxf(fmaxf(b, s[12]), s[13]);
  a = fmaxf(fmaxf(a, s[14]), s[15]);
  return fmaxf(a, b);
}
DI float max_halves(float v) {
  auto r = __builtin_amdgcn_permlane32_swap(__float_as_uint(v), __float_as_uint(v), false, false);
  return fmaxf(__uint_as_float(r[0]), __uint_as_float(r[1]));
}
DI float sum_halves(float v) {
  auto r = __builtin_amdgcn_permlane32_swap(__float_as_uint(v), __float_as_uint(v), false, false);
  return __uint_as_float(r[0]) + __uint_as_float(r[1]);
}
constexpr float THR_LOG2 = 16.f;
constexpr float THR_RAW = THR_LOG2 / SCL;
struct SmState { float mref, l, fs, nmc; bool moved; };

DI void dma_wo(const K1Ctx& c, int gi) {
  const int pb = gi / 5, e = gi - 5 * pb;
  const unsigned ch = (unsigned)(64 * e + c.lane);
  const unsigned row = (ch * 205u) >> 11;
  glds16<true>(c.wob + (size_t)pb * 32 * 1280, row * 1280u + (ch - 10u * row) * 16u, c.lds0 + WO1 + 1024u * (unsigned)gi);
}
constexpr int wo_first(int s) { return s < 0 ? 0 : s > 10 ? 10 : s; }
constexpr int kv_extra(int s) { return s < 0 ? 0 : wo_first(s + 1) - wo_first(s); }
constexpr int kv_wait(int n) {
  int r = n == 0 ? 8 : 0;
  if (n > 0) for (int m = n + 1; m <= n + 3 && m <= 15; ++m) r += 2;
  for (int s = n - 4; s < n; ++s) r += kv_extra(s);
  return r;
}
template <int N>
DI void step_issue(const K1Ctx& c) {
  if constexpr (N >= 1 && N + 4 <= 15) dma_kv2<N + 4>(c);
#pragma unroll
  for (int j = wo_first(N); j < wo_first(N + 1); ++j) dma_wo(c, c.w + 8 * j);
}
template <int N>
DI void step_sync(const K1Ctx& c) {
  wait_bar<kv_wait(N)>();
}
template <bool FIRST>
DI void sm_decide(const f32x16& s, SmState& st) {
  const float tm = max_halves(max16(s));
  if constexpr (FIRST) {
    st.mref = tm;
    st.fs = 1.f;
    st.moved = false;
  } else {
    st.fs = 1.f;
    st.moved = false;
    if (__builtin_expect(__any(tm - st.mref > THR_RAW), 0)) {
      const float mn = fmaxf(st.mref, tm);
      st.fs = __builtin_amdgcn_exp2f((st.mref - mn) * SCL);
      st.mref = mn;
      st.moved = true;
    }
  }
  st.nmc = -st.mref * SCL;
}
DI unsigned sm_pair(const f32x16& s, int i, float nmc, float& la, float& lb) {
  const float e0 = __builtin_amdgcn_exp2f(__builtin_fmaf(s[2 * i], SCL, nmc));
  const float e1 = __builtin_amdgcn_exp2f(__builtin_fmaf(s[2 * i + 1], SCL, nmc));
  la += e0;
  lb += e1;
  asm volatile("" : "+v"(la), "+v"(lb));
  return cvtpk(e0, e1);
}
template <int MODE>
DI void sm_slot(int i, const f32x16& so, float nmc, float& la, float& lb, u32x4& w0, u32x4& w1) {
  if constexpr (MODE == 3) {
    const unsigned wd = sm_pair(so, i, nmc, la, lb);
    if (i < 4) w0[i] = wd; else w1[i - 4] = wd;
  } else if constexpr (MODE == 1) {
    if (i & 1) w0[i >> 1] = sm_pair(so, i >> 1, nmc, la, lb);
  } else if constexpr (MODE == 2) {
    if (i & 1) w1[i >> 1] = sm_pair(so, 4 + (i >> 1), nmc, la, lb);
  }
}
template <int MODE>
DI void sm_sum(SmState& st, float la, float lb) {
  if constexpr (MODE == 1 || MODE == 3) st.l = st.l * st.fs + (la + lb);
  else if constexpr (MODE == 2) st.l += la + lb;
}
template <int N, int MODE>
DI void k_step(const K1Ctx& c, lds_cptr lds, unsigned koff, const bf16x8 (&qf)[8], f32x16& sn, const f32x16& so,
               SmState& st, u32x4& w0, u32x4& w1) {
  step_sync<N>(c);
  unsigned kb = koff + kvaddr(N);
  asm volatile("" : "+v"(kb));
  bf16x8 kf[8];
#pragma unroll
  for (int ks = 0; ks < 8; ++ks) kf[ks] = ldsr128(lds + (kb ^ (unsigned)(ks * 32)));
  step_issue<N>(c);
  float la = 0.f, lb = 0.f;
  const float nmc = st.nmc;
  sn = f32x16{};
  SBAR();
#pragma unroll
  for (int ks = 0; ks < 8; ++ks) {
    sn = MFMA32(kf[ks], qf[ks], sn);
    sm_slot<MODE>(ks, so, nmc, la, lb, w0, w1);
    SBAR();
  }
  sm_sum<MODE>(st, la, lb);
}
template <int N, int MODE>
DI void v_step(const K1Ctx& c, lds_cptr lds, unsigned voff, const u32x4& p0, const u32x4& p1, float fs, bool moved,
               f32x16 (&o)[4], const f32x16& so, SmState& st, u32x4& w0, u32x4& w1) {
  step_sync<N>(c);
  unsigned vb = voff + kvaddr(N);
  asm volatile("" : "+v"(vb));
  bf16x8 vf[8];
#pragma unroll
  for (int dt = 0; dt < 4; ++dt) {
    lds_cptr vp = lds + (vb ^ (unsigned)(dt * 64));
    vf[2 * dt] = cat8(vtr(vp), vtr(vp + 2048));
    vf[2 * dt + 1] = cat8(vtr(vp + 4096), vtr(vp + 4096 + 2048));
  }
  step_issue<N>(c);
  if (__builtin_expect(moved, 0)) {
#pragma unroll
    for (int dt = 0; dt < 4; ++dt) o[dt] *= fs;
  }
  float la = 0.f, lb = 0.f;
  const float nmc = st.nmc;
  SBAR();
#pragma unroll
  for (int i = 0; i < 8; ++i) {
    o[i >> 1] = MFMA32(vf[i], __builtin_bit_cast(bf16x8, (i & 1) ? p1 : p0), o[i >> 1]);
    sm_slot<MODE>(i, so, nmc, la, lb, w0, w1);
    SBAR();
  }
  sm_sum<MODE>(st, la, lb);
}

DI unsigned ld_sc1(const char* p) { return __hip_atomic_load((const unsigned*)p, __ATOMIC_RELAXED, __HIP_MEMORY_SCOPE_AGENT); }
DI void st_sc1(char* p, unsigned v) { __hip_atomic_store((unsigned*)p, v, __ATOMIC_RELAXED, __HIP_MEMORY_SCOPE_AGENT); }

template <int LO, int NP>
DI unsigned handoff_wait(char* ws, int lane, unsigned my_off, unsigned peer_off, unsigned r, unsigned v) {
  const bool live = lane >= LO && lane < LO + NP;
  if (live) st_sc1(ws + WS_ACK + peer_off, v);
  for (int spin = 0; spin < (1 << 22); ++spin) {
    const unsigned a = ld_sc1(ws + WS_ACK + my_off);
    const unsigned vq = ld_sc1(ws + WS_REQ + my_off);
    if (live && vq != v) st_sc1(ws + WS_ACK + peer_off, vq);
    v = vq;
    if (__all(!live || a == r)) break;
    __builtin_amdgcn_s_sleep(1);
  }
  return v;
}
template <int LO, int NP>
DI void handoff_finish(char* ws, int lane, unsigned my_off, unsigned peer_off, unsigned v) {
  const bool live = lane >= LO && lane < LO + NP;
  const unsigned vq = ld_sc1(ws + WS_REQ + my_off);
  if (live && vq != v) st_sc1(ws + WS_ACK + peer_off, vq);
}

__global__ __launch_bounds__(512, 2) void gqa_fused(const char* __restrict__ x, const char* __restrict__ kc,
                                                     const char* __restrict__ vc, const char* __restrict__ wq,
                                                     const char* __restrict__ wo, char* __restrict__ ws,
                                                     unsigned short* __restrict__ out) {
  extern __shared__ __attribute__((aligned(16))) char lds_raw[];
  const lds_ptr lds = (lds_ptr)lds_raw;
  const int tid = threadIdx.x, lane = tid & 63;
  const int w = __builtin_amdgcn_readfirstlane(tid >> 6);
  const int qt = w & 3, g = w >> 2;
  const int bid = blockIdx.x, xcd = bid & 7, idx = bid >> 3;
  const int h = (xcd & 3) * 8 + (idx & 7), b = (xcd >> 2) * 4 + (idx >> 3);
  const int k8 = h >> 2, bp = b >> 1, nt = (b & 1) * 4 + (h & 3);
  const int r32 = lane & 31, hh = lane >> 5, blk = (lane >> 4) & 1, q4 = (lane & 15) >> 2, p = lane & 3;

  const int sl = lane & 15;
  const unsigned my_off = (unsigned)((b * 32 + h) * 64 + 4 * sl);
  const unsigned peer_off = sl < 8 ? (unsigned)(((2 * bp + (sl >> 2)) * 32 + 4 * k8 + (sl & 3)) * 64 + 4 * nt)
                                   : (unsigned)((b * 32 + 4 * (sl - 8) + (h & 3)) * 64 + 32 + 4 * k8);
  unsigned rq = 0;
  if (w == 7) asm volatile("global_load_dword %0, %1, %2 sc1" : "=v"(rq) : "v"(my_off), "s"(ws + WS_ACK) : "memory");

  K1Ctx c;
  c.xb = x + (size_t)b * SQ * DM * 2;
  c.wqb = wq + h * 256;
  c.kb = kc + (size_t)(b * NH + h) * SK * DH * 2;
  c.vb = vc + (size_t)(b * NH + h) * SK * DH * 2;
  c.wob = wo + (size_t)k8 * 512 * 1280 + nt * 160;
  c.w = w;
  c.lane = lane;
  {
    const int xr0 = 16 * w + (lane >> 3), xr1 = xr0 + 8;
    c.vx0 = xr0 < SQ ? (unsigned)(xr0 * 1280 + (((lane & 7) ^ ((xr0 >> 1) & 7)) << 4)) : 0u;
    c.vx1 = xr1 < SQ ? (unsigned)(xr1 * 1280 + (((lane & 7) ^ ((xr1 >> 1) & 7)) << 4)) : 0u;
    const int r0 = 8 * w + (lane >> 4), r1 = r0 + 4;
    c.vw0 = (unsigned)(r0 * 8192 + (((lane & 15) ^ ((r0 & 3) << 2)) << 4));
    c.vw1 = (unsigned)(r1 * 8192 + (((lane & 15) ^ ((r1 & 3) << 2)) << 4));
    c.vk0 = (unsigned)(r0 * 256 + (((lane & 15) ^ (r0 & 15)) << 4));
    c.vk1 = (unsigned)(r1 * 256 + (((lane & 15) ^ (r1 & 15)) << 4));
    c.vv0 = (unsigned)(r0 * 256 + (((lane & 15) ^ ((r0 & 3) << 2)) << 4));
    c.vv1 = (unsigned)(r1 * 256 + (((lane & 15) ^ ((r1 & 3) << 2)) << 4));
  }
  c.lds0 = (unsigned)(uintptr_t)lds_raw;
  c.ldsw = c.lds0 + (unsigned)w * 2048u;
  c.rot = (((b & 3) * 5) / 2 + ((h & 7) * 5) / 4) % 10;

  dma_s<0, 0>(c); dma_s<0, 1>(c); dma_s<0, 2>(c); dma_s<0, 3>(c);
  dma_s<1, 0>(c); dma_s<1, 1>(c); dma_s<1, 2>(c); dma_s<1, 3>(c);
  dma_s<2, 0>(c); dma_s<2, 1>(c); dma_s<2, 2>(c); dma_s<2, 3>(c);
  unsigned tl = 0;
  if (w == 6) {
    const char* tp = lane == 0 ? c.kb : lane == 1 ? c.vb : lane == 2 ? c.wob : lane == 3 ? c.wob + 511 * 1280
                   : lane < 8 ? (const char*)out + (size_t)(2 * bp + (lane & 1)) * SQ * DM * 2
                              : ws + (size_t)min((lane - 8) * 524288, (int)(WS_PART + WS_PART_BYTES) - 64);
    asm volatile("global_load_dword %0, %1, off" : "=v"(tl) : "v"(tp) : "memory");
  }

  f32x16 q0 = {}, q1 = {};
  {
    const int xrow = 32 * qt + r32;
    const unsigned xoff = (unsigned)(xrow * 128 + ((hh ^ ((xrow >> 1) & 7)) << 4));
    const int sp = ((p & 1) << 1) | (p >> 1);
    const int d0 = 2 * g, d1 = 2 * g + 1;
    const unsigned wrow = (unsigned)((8 * hh + q4) * 256 + 8 * (sp & 1));
    const unsigned woff0 = wrow + (unsigned)((4 * (d0 ^ q4) + 2 * blk + (sp >> 1)) << 4);
    const unsigned woff1 = wrow + (unsigned)((4 * (d1 ^ q4) + 2 * blk + (sp >> 1)) << 4);
    wait_bar<4>();
    asm volatile("" : "+v"(rq));
    rq += 1u;
    if (w == 7 && lane < 16) st_sc1(ws + WS_REQ + peer_off, rq);
    PFrag f0 = proj_load<0, 0>(lds, xoff, woff0, woff1);
    PFrag f1 = proj_load<0, 1>(lds, xoff, woff0, woff1);
    proj_stage<0>(c, lds, xoff, woff0, woff1, q0, q1, f0, f1);
    proj_stage<1>(c, lds, xoff, woff0, woff1, q0, q1, f0, f1);
    asm volatile("" ::"v"(tl));
    proj_stage<2>(c, lds, xoff, woff0, woff1, q0, q1, f0, f1);
    proj_stage<3>(c, lds, xoff, woff0, woff1, q0, q1, f0, f1);
    proj_stage<4>(c, lds, xoff, woff0, woff1, q0, q1, f0, f1);
    proj_stage<5>(c, lds, xoff, woff0, woff1, q0, q1, f0, f1);
    proj_stage<6>(c, lds, xoff, woff0, woff1, q0, q1, f0, f1);
    proj_stage<7>(c, lds, xoff, woff0, woff1, q0, q1, f0, f1);
    proj_stage<8>(c, lds, xoff, woff0, woff1, q0, q1, f0, f1);
    proj_stage<9>(c, lds, xoff, woff0, woff1, q0, q1, f0, f1);
  }
  bf16x8 qf[8];
  {
    lds_ptr e = lds + XSLOT * SLOT + qt * 8192 + g * 4096 + lane * 16;
    *(__attribute__((address_space(3))) bf16x8*)(e) = pack8<0>(q0);
    *(__attribute__((address_space(3))) bf16x8*)(e + 1024) = pack8<1>(q0);
    *(__attribute__((address_space(3))) bf16x8*)(e + 2048) = pack8<0>(q1);
    *(__attribute__((address_space(3))) bf16x8*)(e + 3072) = pack8<1>(q1);
  }
  lds_bar();
  dma_kv2<4>(c);
  {
    lds_cptr e = lds + XSLOT * SLOT + qt * 8192 + lane * 16;
#pragma unroll
    for (int ks = 0; ks < 8; ++ks) qf[ks] = ldsr128(e + ks * 1024);
  }
  f32x16 o[4] = {};
  SmState st;
  st.l = 0.f;
  float m, l;
  {
    const int krow = 32 * g + r32;
    const unsigned koff = (unsigned)(krow * 256 + ((hh ^ (krow & 15)) << 4));
    const unsigned voff = (unsigned)((32 * g + 4 * hh + q4) * 256 + 8 * (p & 1) + ((4 * q4 + 2 * blk + (p >> 1)) << 4));
    f32x16 sA, sB;
    u32x4 pa0, pa1, pb0, pb1;
    float fsp; bool mvp;
#define KSTEP(N, SN, SO, W0, W1)                                   \
    fsp = st.fs; mvp = st.moved;                                   \
    k_step<N, 2>(c, lds, koff, qf, SN, SO, st, W0, W1);            \
    sm_decide<false>(SN, st);
#define VSTEP(N, P0, P1, SO, W0, W1) v_step<N, 1>(c, lds, voff, P0, P1, fsp, mvp, o, SO, st, W0, W1);
    k_step<0, 0>(c, lds, koff, qf, sA, sA, st, pa0, pa1);
    sm_decide<true>(sA, st);
    fsp = 1.f; mvp = false;
    k_step<1, 3>(c, lds, koff, qf, sB, sA, st, pa0, pa1);
    sm_decide<false>(sB, st);
    VSTEP(2, pa0, pa1, sB, pb0, pb1)   KSTEP(3, sA, sB, pb0, pb1)
    VSTEP(4, pb0, pb1, sA, pa0, pa1)   KSTEP(5, sB, sA, pa0, pa1)
    VSTEP(6, pa0, pa1, sB, pb0, pb1)   KSTEP(7, sA, sB, pb0, pb1)
    VSTEP(8, pb0, pb1, sA, pa0, pa1)   KSTEP(9, sB, sA, pa0, pa1)
    VSTEP(10, pa0, pa1, sB, pb0, pb1)  KSTEP(11, sA, sB, pb0, pb1)
    VSTEP(12, pb0, pb1, sA, pa0, pa1)  KSTEP(13, sB, sA, pa0, pa1)
    {
      const float fs7 = st.fs; const bool mv7 = st.moved;
      v_step<14, 3>(c, lds, voff, pa0, pa1, fsp, mvp, o, sB, st, pb0, pb1);
      m = st.mref;
      l = sum_halves(st.l);
      *(__attribute__((address_space(3))) f32x2*)(lds + 2 * SLOT + w * 512 + lane * 8) = f32x2{m, l};
      v_step<15, 0>(c, lds, voff, pb0, pb1, fs7, mv7, o, sA, st, pa0, pa1);
    }
#undef KSTEP
#undef VSTEP
  }
  {
    const int w2 = w ^ 4;
    const f32x2 ml2 = *(const __attribute__((address_space(3))) f32x2*)(lds + 2 * SLOT + w2 * 512 + lane * 8);
    lds_bar();
    lds_ptr mb = lds + w * 8192 + lane * 16;
    f32x16 sa, sb, ka, kb2;
    if (g == 0) { sa = o[2]; sb = o[3]; ka = o[0]; kb2 = o[1]; }
    else        { sa = o[0]; sb = o[1]; ka = o[2]; kb2 = o[3]; }
#pragma unroll
    for (int i = 0; i < 4; ++i) {
      *(__attribute__((address_space(3))) f32x4*)(mb + i * 1024) = f32x4{sa[4 * i], sa[4 * i + 1], sa[4 * i + 2], sa[4 * i + 3]};
      *(__attribute__((address_space(3))) f32x4*)(mb + 4096 + i * 1024) = f32x4{sb[4 * i], sb[4 * i + 1], sb[4 * i + 2], sb[4 * i + 3]};
    }
    const float mt = fmaxf(m, ml2[0]);
    const float a1 = __builtin_amdgcn_exp2f((m - mt) * SCL), a2 = __builtin_amdgcn_exp2f((ml2[0] - mt) * SCL);
    const float inv = 1.0f / (a1 * l + a2 * ml2[1]);
    const float f1 = a1 * inv, f2 = a2 * inv;
    lds_bar();
    lds_cptr pb = lds + w2 * 8192 + lane * 16;
#pragma unroll
    for (int i = 0; i < 4; ++i) {
      const f32x4 u0 = *(const __attribute__((address_space(3))) f32x4*)(pb + i * 1024);
      const f32x4 u1 = *(const __attribute__((address_space(3))) f32x4*)(pb + 4096 + i * 1024);
#pragma unroll
      for (int j = 0; j < 4; ++j) {
        ka[4 * i + j] = ka[4 * i + j] * f1 + u0[j] * f2;
        kb2[4 * i + j] = kb2[4 * i + j] * f1 + u1[j] * f2;
      }
    }
    const int m16 = 2 * qt + (r32 >> 4);
    if (m16 < 7) {
      char* ob = ws + WS_O + ((size_t)((b * 128 + h * 4 + 2 * g) * 7 + m16)) * 1024 + (16 * hh + (r32 & 15)) * 16;
      auto pk = [](const f32x16& v, int u) {
        u32x4 t = {cvtpk(v[4 * u], v[4 * u + 1]), cvtpk(v[4 * u + 2], v[4 * u + 3]), cvtpk(v[4 * u + 8], v[4 * u + 9]),
                   cvtpk(v[4 * u + 10], v[4 * u + 11])};
        return __builtin_bit_cast(bf16x8, t);
      };
      store_wt(ob, pk(ka, 0));
      store_wt(ob + 512, pk(ka, 1));
      store_wt(ob + 7 * 1024, pk(kb2, 0));
      store_wt(ob + 7 * 1024 + 512, pk(kb2, 1));
    }
  }
  unsigned rv = 0;
  if (w == 7) rv = ld_sc1(ws + WS_REQ + my_off);
  wait_bar<0>();
  if (w == 7) rv = handoff_wait<0, 8>(ws, lane, my_off, peer_off, rq, rv);
  wait_bar<0>();
  const int gg = lane >> 4;
  const auto prs = __builtin_amdgcn_make_buffer_rsrc((void*)(ws + WS_PART + (size_t)(bp * 8 + nt) * 8 * 14 * 2560), 0,
                                                     8 * 14 * 2560, 0x00020000);
  f32x4 acc[2][5];
#pragma unroll
  for (int i = 0; i < 2; ++i)
#pragma unroll
    for (int j = 0; j < 5; ++j) acc[i][j] = f32x4{0.f, 0.f, 0.f, 0.f};
  if (w < 7) {
    const auto ors = __builtin_amdgcn_make_buffer_rsrc((void*)(ws + WS_O + (size_t)(2 * bp * 128 + 16 * k8) * 7 * 1024), 0,
                                                       (128 + 16) * 7 * 1024, 0x00020000);
    const int mo = w * 1024 + lane * 16;
    const unsigned wl = (unsigned)(WO1 + (4 * gg + q4) * 160 + 8 * p);
    constexpr int PD = 4;
    const int rot = 2 * nt;
    u32x4 of[PD][2];
#pragma unroll
    for (int ks = 0; ks < PD; ++ks) {
      of[ks][0] = __builtin_amdgcn_raw_buffer_load_b128(ors, mo, ((ks + rot) & 15) * 7168, 16);
      of[ks][1] = __builtin_amdgcn_raw_buffer_load_b128(ors, mo, (128 + ((ks + rot) & 15)) * 7168, 16);
    }
    SBAR();
#pragma unroll
    for (int ks = 0; ks < 16; ++ks) {
      lds_cptr wp = lds + (wl + (unsigned)(((ks + rot) & 15) * 5120));
      bf16x8 wf[5];
#pragma unroll
      for (int j = 0; j < 5; ++j) wf[j] = cat8(vtr(wp + 32 * j), vtr(wp + 32 * j + 2560));
      const bf16x8 o0 = __builtin_bit_cast(bf16x8, of[ks % PD][0]), o1 = __builtin_bit_cast(bf16x8, of[ks % PD][1]);
#pragma unroll
      for (int j = 0; j < 5; ++j) {
        acc[0][j] = MFMA16(wf[j], o0, acc[0][j]);
        acc[1][j] = MFMA16(wf[j], o1, acc[1][j]);
      }
      if (ks + PD < 16) {
        of[ks % PD][0] = __builtin_amdgcn_raw_buffer_load_b128(ors, mo, ((ks + PD + rot) & 15) * 7168, 16);
        of[ks % PD][1] = __builtin_amdgcn_raw_buffer_load_b128(ors, mo, (128 + ((ks + PD + rot) & 15)) * 7168, 16);
      }
      SBAR();
    }
#pragma unroll
    for (int i = 0; i < 2; ++i) {
      if (!(gg == (k8 & 3) && i == (k8 >> 2))) {
        const int po = (k8 * 14 + i * 7 + w) * 2560;
        const u32x4 ra = {cvtpk(acc[i][0][0], acc[i][0][1]), cvtpk(acc[i][0][2], acc[i][0][3]),
                          cvtpk(acc[i][1][0], acc[i][1][1]), cvtpk(acc[i][1][2], acc[i][1][3])};
        const u32x4 rb = {cvtpk(acc[i][2][0], acc[i][2][1]), cvtpk(acc[i][2][2], acc[i][2][3]),
                          cvtpk(acc[i][3][0], acc[i][3][1]), cvtpk(acc[i][3][2], acc[i][3][3])};
        const u32x2 rc = {cvtpk(acc[i][4][0], acc[i][4][1]), cvtpk(acc[i][4][2], acc[i][4][3])};
        __builtin_amdgcn_raw_buffer_store_b128(ra, prs, po + lane * 16, 0, 16);
        __builtin_amdgcn_raw_buffer_store_b128(rb, prs, po + 1024 + lane * 16, 0, 16);
        __builtin_amdgcn_raw_buffer_store_b64(rc, prs, po + 2048 + lane * 8, 0, 16);
      }
    }
  } else {
    handoff_finish<0, 8>(ws, lane, my_off, peer_off, rv);
    rv = ld_sc1(ws + WS_REQ + my_off);
  }
  wait_bar<0>();
  if (w == 7) rv = handoff_wait<8, 8>(ws, lane, my_off, peer_off, rq, rv);
  wait_bar<0>();
  if (w == 7) handoff_finish<8, 8>(ws, lane, my_off, peer_off, rv);
  if (w < 7) {
    const int ih = k8 >> 2;
    const int sl2 = 16 * (k8 & 3) + (lane & 15);
    u32x4 ua[2], ub[2];
    u32x2 uc[2];
#pragma unroll
    for (int q = 0; q < 2; ++q) {
      const int po = ((gg + 4 * q) * 14 + ih * 7 + w) * 2560;
      ua[q] = __builtin_amdgcn_raw_buffer_load_b128(prs, po + sl2 * 16, 0, 16);
      ub[q] = __builtin_amdgcn_raw_buffer_load_b128(prs, po + 1024 + sl2 * 16, 0, 16);
      uc[q] = __builtin_amdgcn_raw_buffer_load_b64(prs, po + 2048 + sl2 * 8, 0, 16);
    }
    auto unpk = [](unsigned lo, unsigned hi) {
      return f32x4{__uint_as_float(lo << 16), __uint_as_float(lo & 0xffff0000u), __uint_as_float(hi << 16),
                   __uint_as_float(hi & 0xffff0000u)};
    };
    f32x4 r[5];
#pragma unroll
    for (int j = 0; j < 5; ++j) {
      const f32x4 own = ih ? acc[1][j] : acc[0][j];
      f32x4 v2[2];
#pragma unroll
      for (int q = 0; q < 2; ++q) {
        const f32x4 pv = j == 0 ? unpk(ua[q][0], ua[q][1]) : j == 1 ? unpk(ua[q][2], ua[q][3])
                       : j == 2 ? unpk(ub[q][0], ub[q][1]) : j == 3 ? unpk(ub[q][2], ub[q][3]) : unpk(uc[q][0], uc[q][1]);
        v2[q] = gg + 4 * q == k8 ? own : pv;
      }
      f32x4 t = v2[0] + v2[1];
#pragma unroll
      for (int e = 0; e < 4; ++e) {
        t[e] += __shfl_xor(t[e], 16);
        t[e] += __shfl_xor(t[e], 32);
      }
      r[j] = t;
    }
    const f32x4 t0 = gg == 0 ? r[0] : gg == 1 ? r[1] : gg == 2 ? r[2] : r[3];
    unsigned short* op = out + (size_t)((2 * bp + ih) * SQ + 16 * w + (lane & 15)) * DM + 80 * nt + 4 * (k8 & 3);
    *(u32x2*)(op + 16 * gg) = u32x2{cvtpk(t0[0], t0[1]), cvtpk(t0[2], t0[3])};
    if (gg == 0) *(u32x2*)(op + 64) = u32x2{cvtpk(r[4][0], r[4][1]), cvtpk(r[4][2], r[4][3])};
  }
}

}

extern "C" void kernel_launch(void* const* d_in, const int* in_sizes, int n_in, void* d_out, int out_size, void* d_ws,
                              size_t ws_size, hipStream_t stream) {
  (void)in_sizes; (void)n_in; (void)out_size; (void)ws_size;
  (void)hipFuncSetAttribute((const void*)gqa_fused, hipFuncAttributeMaxDynamicSharedMemorySize, LDS1);
  hipLaunchKernelGGL(gqa_fused, dim3(256), dim3(512), LDS1, stream, (const char*)d_in[0], (const char*)d_in[1],
                     (const char*)d_in[2], (const char*)d_in[3], (const char*)d_in[4], (char*)d_ws,
                     (unsigned short*)d_out);
}
```

```cpp
#include <hip/hip_runtime.h>
#include <hip/hip_bf16.h>
#include <stdint.h>

namespace {

using bf16x8 = __attribute__((ext_vector_type(8))) short;
using s16x4 = __attribute__((ext_vector_type(4))) short;
using f32x16 = __attribute__((ext_vector_type(16))) float;
using f32x4 = __attribute__((ext_vector_type(4))) float;
using f32x2 = __attribute__((ext_vector_type(2))) float;
using u32x4 = __attribute__((ext_vector_type(4))) unsigned;
using u32x2 = __attribute__((ext_vector_type(2))) unsigned;
typedef short v4i16_t __attribute__((ext_vector_type(4)));
typedef __attribute__((address_space(3))) char* lds_ptr;
typedef __attribute__((address_space(3))) const char* lds_cptr;

#define DI __device__ __forceinline__
#define SBAR() __builtin_amdgcn_sched_barrier(0)
#define MFMA32(a, b, c) __builtin_amdgcn_mfma_f32_32x32x16_bf16((a), (b), (c), 0, 0, 0)
#define MFMA16(a, b, c) __builtin_amdgcn_mfma_f32_16x16x32_bf16((a), (b), (c), 0, 0, 0)

constexpr int NB = 8, SQ = 112, DM = 640, NH = 32, SK = 512, DH = 128;
constexpr int SLOT = 16384;
constexpr int LDS1 = 10 * SLOT;
constexpr float SCL = 0.08838834764831845f * 1.4426950408889634f;

constexpr size_t WS_REQ = 0;
constexpr size_t WS_ACK = 16384;
constexpr size_t WS_O = 32768;
constexpr size_t WS_O_BYTES = (size_t)NB * 128 * 7 * 1024;
constexpr size_t WS_PART = WS_O + WS_O_BYTES;
constexpr size_t WS_PART_BYTES = (size_t)32 * 8 * 14 * 2560;

DI s16x4 vtr(lds_cptr p) {
  return __builtin_bit_cast(s16x4, __builtin_amdgcn_ds_read_tr16_b64_v4i16((__attribute__((address_space(3))) v4i16_t*)p));
}
DI bf16x8 ldsr128(lds_cptr p) { return *(const __attribute__((address_space(3))) bf16x8*)p; }
DI bf16x8 cat8(s16x4 lo, s16x4 hi) { return __builtin_shufflevector(lo, hi, 0, 1, 2, 3, 4, 5, 6, 7); }
typedef __bf16 bfv2 __attribute__((ext_vector_type(2)));
DI unsigned cvtpk(float lo, float hi) {
  const f32x2 v = {lo, hi};
  return __builtin_bit_cast(unsigned, __builtin_convertvector(v, bfv2));
}
template <int S>
DI bf16x8 pack8(const f32x16& x) {
  u32x4 p = {cvtpk(x[8 * S + 0], x[8 * S + 1]), cvtpk(x[8 * S + 2], x[8 * S + 3]), cvtpk(x[8 * S + 4], x[8 * S + 5]),
             cvtpk(x[8 * S + 6], x[8 * S + 7])};
  return __builtin_bit_cast(bf16x8, p);
}
template <bool NT>
DI void glds16(const void* sbase, unsigned voff, unsigned lds_dst) {
  unsigned sv;
  if constexpr (NT)
    asm volatile("s_mov_b32 %0, m0\n\ts_mov_b32 m0, %3\n\ts_nop 2\n\tglobal_load_lds_dwordx4 %1, %2 nt\n\ts_mov_b32 m0, %0"
                 : "=&s"(sv) : "v"(voff), "s"(sbase), "s"(lds_dst) : "memory");
  else
    asm volatile("s_mov_b32 %0, m0\n\ts_mov_b32 m0, %3\n\ts_nop 2\n\tglobal_load_lds_dwordx4 %1, %2\n\ts_mov_b32 m0, %0"
                 : "=&s"(sv) : "v"(voff), "s"(sbase), "s"(lds_dst) : "memory");
}
template <int N>
DI void wait_bar() {
  asm volatile("s_waitcnt vmcnt(%0) lgkmcnt(0)\n\ts_barrier" ::"i"(N) : "memory");
}
DI void store_wt(char* p, bf16x8 v) {
  asm volatile("global_store_dwordx4 %0, %1, off sc1\n\ts_nop 1" ::"v"(p), "v"(v) : "memory");
}
DI void lds_bar() { asm volatile("s_waitcnt lgkmcnt(0)\n\ts_barrier" ::: "memory"); }

constexpr int sslot(int i) { return (2 * i) % 10; }
constexpr int kvaddr(int it) { return it % 5 < 4 ? (it % 5) * SLOT : 9 * SLOT; }
constexpr int XSLOT = 6;
constexpr int WO1 = 65536;

struct K1Ctx {
  const char* xb;
  const char* wqb;
  const char* kb;
  const char* vb;
  unsigned vx0, vx1, vw0, vw1, vk0, vk1, vv0, vv1;
  unsigned ldsw;
  int rot;
  const char* wob;
  unsigned lds0;
  int w, lane;
};

template <int I, int J>
DI void dma_s(const K1Ctx& c) {
  constexpr int sl = sslot(I);
  int st = I + c.rot;
  st = st >= 10 ? st - 10 : st;
  if constexpr (J == 0) glds16<false>(c.xb + st * 128, c.vx0, c.ldsw + sl * SLOT);
  if constexpr (J == 1) glds16<false>(c.wqb + (size_t)st * 64 * 8192, c.vw0, c.ldsw + (sl + 1) * SLOT);
  if constexpr (J == 2) glds16<false>(c.xb + st * 128, c.vx1, c.ldsw + sl * SLOT + 1024);
  if constexpr (J == 3) glds16<false>(c.wqb + (size_t)st * 64 * 8192, c.vw1, c.ldsw + (sl + 1) * SLOT + 1024);
}
constexpr bool pos_is_k(int n) { return n == 0 || ((n & 1) && n < 15); }
constexpr int pos_tile(int n) { return n == 0 ? 0 : (n == 15 ? 7 : ((n & 1) ? (n + 1) / 2 : n / 2 - 1)); }
template <int IT, int J>
DI void dma_kv(const K1Ctx& c) {
  if constexpr (pos_is_k(IT))
    glds16<true>(c.kb + pos_tile(IT) * 16384, J ? c.vk1 : c.vk0, c.ldsw + kvaddr(IT) + J * 1024);
  else
    glds16<true>(c.vb + pos_tile(IT) * 16384, J ? c.vv1 : c.vv0, c.ldsw + kvaddr(IT) + J * 1024);
}
template <int IT>
DI void dma_kv2(const K1Ctx& c) { dma_kv<IT, 0>(c); dma_kv<IT, 1>(c); }
constexpr int RA = 4;
constexpr int PDIST = 3;
template <int I, int J>
DI void dma_in_stage(const K1Ctx& c) {
  if constexpr (I + PDIST <= 9) dma_s<I + PDIST, J>(c);
  else if constexpr ((I - 7) * 2 + J / 2 < RA) dma_kv<(I - 7) * 2 + J / 2, J % 2>(c);
}
constexpr int proj_wait(int I) {
  int n = 0;
  for (int st = I + 3; st <= I + PDIST && st <= 9; ++st) n += 4;
  if (I == 7) n += 4;
  return n;
}

struct PFrag { bf16x8 x, a0, a1; };
template <int I, int KS>
DI PFrag proj_load(lds_cptr lds, unsigned xoff, unsigned woff0, unsigned woff1) {
  lds_cptr xs = lds + sslot(I) * SLOT, ws = lds + (sslot(I) + 1) * SLOT;
  PFrag f;
  f.x = ldsr128(xs + (xoff ^ (unsigned)(KS * 32)));
  f.a0 = cat8(vtr(ws + woff0 + KS * 4096), vtr(ws + woff0 + KS * 4096 + 1024));
  f.a1 = cat8(vtr(ws + woff1 + KS * 4096), vtr(ws + woff1 + KS * 4096 + 1024));
  return f;
}
template <int I>
DI void proj_stage(const K1Ctx& c, lds_cptr lds, unsigned xoff, unsigned woff0, unsigned woff1, f32x16& q0, f32x16& q1,
                   PFrag& f0, PFrag& f1) {
  PFrag f2 = proj_load<I, 2>(lds, xoff, woff0, woff1);
  SBAR();
  q0 = MFMA32(f0.a0, f0.x, q0);
  q1 = MFMA32(f0.a1, f0.x, q1);
  SBAR();
  dma_in_stage<I, 0>(c);
  PFrag f3 = proj_load<I, 3>(lds, xoff, woff0, woff1);
  SBAR();
  q0 = MFMA32(f1.a0, f1.x, q0);
  q1 = MFMA32(f1.a1, f1.x, q1);
  SBAR();
  dma_in_stage<I, 1>(c);
  if constexpr (I < 9) f0 = proj_load<I + 1, 0>(lds, xoff, woff0, woff1);
  SBAR();
  q0 = MFMA32(f2.a0, f2.x, q0);
  q1 = MFMA32(f2.a1, f2.x, q1);
  SBAR();
  dma_in_stage<I, 2>(c);
  if constexpr (I < 9) f1 = proj_load<I + 1, 1>(lds, xoff, woff0, woff1);
  SBAR();
  q0 = MFMA32(f3.a0, f3.x, q0);
  q1 = MFMA32(f3.a1, f3.x, q1);
  SBAR();
  dma_in_stage<I, 3>(c);
  if constexpr (I <= 7) wait_bar<proj_wait(I)>();
  else if constexpr (I == 8) lds_bar();
}

DI float max16(const f32x16& s) {
  float a = fmaxf(fmaxf(s[0], s[1]), s[2]), b = fmaxf(fmaxf(s[3], s[4]), s[5]);
  a = fmaxf(fmaxf(a, s[6]), s[7]);
  b = fmaxf(fmaxf(b, s[8]), s[9]);
  a = fmaxf(fmaxf(a, s[10]), s[11]);
  b = fmaxf(fmaxf(b, s[12]), s[13]);
  a = fmaxf(fmaxf(a, s[14]), s[15]);
  return fmaxf(a, b);
}
DI float max_halves(float v) {
  auto r = __builtin_amdgcn_permlane32_swap(__float_as_uint(v), __float_as_uint(v), false, false);
  return fmaxf(__uint_as_float(r[0]), __uint_as_float(r[1]));
}
DI float sum_halves(float v) {
  auto r = __builtin_amdgcn_permlane32_swap(__float_as_uint(v), __float_as_uint(v), false, false);
  return __uint_as_float(r[0]) + __uint_as_float(r[1]);
}
constexpr float THR_LOG2 = 16.f;
constexpr float THR_RAW = THR_LOG2 / SCL;
struct SmState { float mref, l, fs, nmc; bool moved; };

DI void dma_wo(const K1Ctx& c, int gi) {
  const int pb = gi / 5, e = gi - 5 * pb;
  const unsigned ch = (unsigned)(64 * e + c.lane);
  const unsigned row = (ch * 205u) >> 11;
  glds16<true>(c.wob + (size_t)pb * 32 * 1280, row * 1280u + (ch - 10u * row) * 16u, c.lds0 + WO1 + 1024u * (unsigned)gi);
}
constexpr int wo_first(int s) { return s < 0 ? 0 : s > 10 ? 10 : s; }
constexpr int kv_extra(int s) { return s < 0 ? 0 : wo_first(s + 1) - wo_first(s); }
constexpr int kv_wait(int n) {
  int r = n == 0 ? 8 : 0;
  if (n > 0) for (int m = n + 1; m <= n + 3 && m <= 15; ++m) r += 2;
  for (int s = n - 4; s < n; ++s) r += kv_extra(s);
  return r;
}
template <int N>
DI void step_issue(const K1Ctx& c) {
  if constexpr (N >= 1 && N + 4 <= 15) dma_kv2<N + 4>(c);
#pragma unroll
  for (int j = wo_first(N); j < wo_first(N + 1); ++j) dma_wo(c, c.w + 8 * j);
}
template <int N>
DI void step_sync(const K1Ctx& c) {
  wait_bar<kv_wait(N)>();
}
template <bool FIRST>
DI void sm_decide(const f32x16& s, SmState& st) {
  const float tm = max_halves(max16(s));
  if constexpr (FIRST) {
    st.mref = tm;
    st.fs = 1.f;
    st.moved = false;
  } else {
    st.fs = 1.f;
    st.moved = false;
    if (__builtin_expect(__any(tm - st.mref > THR_RAW), 0)) {
      const float mn = fmaxf(st.mref, tm);
      st.fs = __builtin_amdgcn_exp2f((st.mref - mn) * SCL);
      st.mref = mn;
      st.moved = true;
    }
  }
  st.nmc = -st.mref * SCL;
}
DI unsigned sm_pair(const f32x16& s, int i, float nmc, float& la, float& lb) {
  const float e0 = __builtin_amdgcn_exp2f(__builtin_fmaf(s[2 * i], SCL, nmc));
  const float e1 = __builtin_amdgcn_exp2f(__builtin_fmaf(s[2 * i + 1], SCL, nmc));
  la += e0;
  lb += e1;
  asm volatile("" : "+v"(la), "+v"(lb));
  return cvtpk(e0, e1);
}
template <int MODE>
DI void sm_slot(int i, const f32x16& so, float nmc, float& la, float& lb, u32x4& w0, u32x4& w1) {
  if constexpr (MODE == 3) {
    const unsigned wd = sm_pair(so, i, nmc, la, lb);
    if (i < 4) w0[i] = wd; else w1[i - 4] = wd;
  } else if constexpr (MODE == 1) {
    if (i & 1) w0[i >> 1] = sm_pair(so, i >> 1, nmc, la, lb);
  } else if constexpr (MODE == 2) {
    if (i & 1) w1[i >> 1] = sm_pair(so, 4 + (i >> 1), nmc, la, lb);
  }
}
template <int MODE>
DI void sm_sum(SmState& st, float la, float lb) {
  if constexpr (MODE == 1 || MODE == 3) st.l = st.l * st.fs + (la + lb);
  else if constexpr (MODE == 2) st.l += la + lb;
}
template <int N, int MODE>
DI void k_step(const K1Ctx& c, lds_cptr lds, unsigned koff, const bf16x8 (&qf)[8], f32x16& sn, const f32x16& so,
               SmState& st, u32x4& w0, u32x4& w1) {
  step_sync<N>(c);
  unsigned kb = koff + kvaddr(N);
  asm volatile("" : "+v"(kb));
  bf16x8 kf[8];
#pragma unroll
  for (int ks = 0; ks < 8; ++ks) kf[ks] = ldsr128(lds + (kb ^ (unsigned)(ks * 32)));
  step_issue<N>(c);
  float la = 0.f, lb = 0.f;
  const float nmc = st.nmc;
  sn = f32x16{};
  SBAR();
#pragma unroll
  for (int ks = 0; ks < 8; ++ks) {
    sn = MFMA32(kf[ks], qf[ks], sn);
    sm_slot<MODE>(ks, so, nmc, la, lb, w0, w1);
    SBAR();
  }
  sm_sum<MODE>(st, la, lb);
}
template <int N, int MODE>
DI void v_step(const K1Ctx& c, lds_cptr lds, unsigned voff, const u32x4& p0, const u32x4& p1, float fs, bool moved,
               f32x16 (&o)[4], const f32x16& so, SmState& st, u32x4& w0, u32x4& w1) {
  step_sync<N>(c);
  unsigned vb = voff + kvaddr(N);
  asm volatile("" : "+v"(vb));
  bf16x8 vf[8];
#pragma unroll
  for (int dt = 0; dt < 4; ++dt) {
    lds_cptr vp = lds + (vb ^ (unsigned)(dt * 64));
    vf[2 * dt] = cat8(vtr(vp), vtr(vp + 2048));
    vf[2 * dt + 1] = cat8(vtr(vp + 4096), vtr(vp + 4096 + 2048));
  }
  step_issue<N>(c);
  if (__builtin_expect(moved, 0)) {
#pragma unroll
    for (int dt = 0; dt < 4; ++dt) o[dt] *= fs;
  }
  float la = 0.f, lb = 0.f;
  const float nmc = st.nmc;
  SBAR();
#pragma unroll
  for (int i = 0; i < 8; ++i) {
    o[i >> 1] = MFMA32(vf[i], __builtin_bit_cast(bf16x8, (i & 1) ? p1 : p0), o[i >> 1]);
    sm_slot<MODE>(i, so, nmc, la, lb, w0, w1);
    SBAR();
  }
  sm_sum<MODE>(st, la, lb);
}

DI unsigned ld_sc1(const char* p) { return __hip_atomic_load((const unsigned*)p, __ATOMIC_RELAXED, __HIP_MEMORY_SCOPE_AGENT); }
DI void st_sc1(char* p, unsigned v) { __hip_atomic_store((unsigned*)p, v, __ATOMIC_RELAXED, __HIP_MEMORY_SCOPE_AGENT); }

template <int LO, int NP>
DI unsigned handoff_wait(char* ws, int lane, unsigned my_off, unsigned peer_off, unsigned r, unsigned v) {
  const bool live = lane >= LO && lane < LO + NP;
  if (live) st_sc1(ws + WS_ACK + peer_off, v);
  for (int spin = 0; spin < (1 << 22); ++spin) {
    const unsigned a = ld_sc1(ws + WS_ACK + my_off);
    const unsigned vq = ld_sc1(ws + WS_REQ + my_off);
    if (live && vq != v) st_sc1(ws + WS_ACK + peer_off, vq);
    v = vq;
    if (__all(!live || a == r)) break;
    __builtin_amdgcn_s_sleep(1);
  }
  return v;
}
template <int LO, int NP>
DI void handoff_finish(char* ws, int lane, unsigned my_off, unsigned peer_off, unsigned v) {
  const bool live = lane >= LO && lane < LO + NP;
  const unsigned vq = ld_sc1(ws + WS_REQ + my_off);
  if (live && vq != v) st_sc1(ws + WS_ACK + peer_off, vq);
}

__global__ __launch_bounds__(512, 2) void gqa_fused(const char* __restrict__ x, const char* __restrict__ kc,
                                                     const char* __restrict__ vc, const char* __restrict__ wq,
                                                     const char* __restrict__ wo, char* __restrict__ ws,
                                                     unsigned short* __restrict__ out) {
  extern __shared__ __attribute__((aligned(16))) char lds_raw[];
  const lds_ptr lds = (lds_ptr)lds_raw;
  const int tid = threadIdx.x, lane = tid & 63;
  const int w = __builtin_amdgcn_readfirstlane(tid >> 6);
  const int qt = w & 3, g = w >> 2;
  const int bid = blockIdx.x, xcd = bid & 7, idx = bid >> 3;
  const int h = (xcd & 3) * 8 + (idx & 7), b = (xcd >> 2) * 4 + (idx >> 3);
  const int k8 = h >> 2, bp = b >> 1, nt = (b & 1) * 4 + (h & 3);
  const int r32 = lane & 31, hh = lane >> 5, blk = (lane >> 4) & 1, q4 = (lane & 15) >> 2, p = lane & 3;

  const int sl = lane & 15;
  const unsigned my_off = (unsigned)((b * 32 + h) * 64 + 4 * sl);
  const unsigned peer_off = sl < 8 ? (unsigned)(((2 * bp + (sl >> 2)) * 32 + 4 * k8 + (sl & 3)) * 64 + 4 * nt)
                                   : (unsigned)((b * 32 + 4 * (sl - 8) + (h & 3)) * 64 + 32 + 4 * k8);
  unsigned rq = 0;
  if (w == 7) asm volatile("global_load_dword %0, %1, %2 sc1" : "=v"(rq) : "v"(my_off), "s"(ws + WS_ACK) : "memory");

  K1Ctx c;
  c.xb = x + (size_t)b * SQ * DM * 2;
  c.wqb = wq + h * 256;
  c.kb = kc + (size_t)(b * NH + h) * SK * DH * 2;
  c.vb = vc + (size_t)(b * NH + h) * SK * DH * 2;
  c.wob = wo + (size_t)k8 * 512 * 1280 + nt * 160;
  c.w = w;
  c.lane = lane;
  {
    const int xr0 = 16 * w + (lane >> 3), xr1 = xr0 + 8;
    c.vx0 = xr0 < SQ ? (unsigned)(xr0 * 1280 + (((lane & 7) ^ ((xr0 >> 1) & 7)) << 4)) : 0u;
    c.vx1 = xr1 < SQ ? (unsigned)(xr1 * 1280 + (((lane & 7) ^ ((xr1 >> 1) & 7)) << 4)) : 0u;
    const int r0 = 8 * w + (lane >> 4), r1 = r0 + 4;
    c.vw0 = (unsigned)(r0 * 8192 + (((lane & 15) ^ ((r0 & 3) << 2)) << 4));
    c.vw1 = (unsigned)(r1 * 8192 + (((lane & 15) ^ ((r1 & 3) << 2)) << 4));
    c.vk0 = (unsigned)(r0 * 256 + (((lane & 15) ^ (r0 & 15)) << 4));
    c.vk1 = (unsigned)(r1 * 256 + (((lane & 15) ^ (r1 & 15)) << 4));
    c.vv0 = (unsigned)(r0 * 256 + (((lane & 15) ^ ((r0 & 3) << 2)) << 4));
    c.vv1 = (unsigned)(r1 * 256 + (((lane & 15) ^ ((r1 & 3) << 2)) << 4));
  }
  c.lds0 = (unsigned)(uintptr_t)lds_raw;
  c.ldsw = c.lds0 + (unsigned)w * 2048u;
  c.rot = (((b & 3) * 5) / 2 + ((h & 7) * 5) / 4) % 10;

  dma_s<0, 0>(c); dma_s<0, 1>(c); dma_s<0, 2>(c); dma_s<0, 3>(c);
  dma_s<1, 0>(c); dma_s<1, 1>(c); dma_s<1, 2>(c); dma_s<1, 3>(c);
  dma_s<2, 0>(c); dma_s<2, 1>(c); dma_s<2, 2>(c); dma_s<2, 3>(c);

  f32x16 q0 = {}, q1 = {};
  {
    const int xrow = 32 * qt + r32;
    const unsigned xoff = (unsigned)(xrow * 128 + ((hh ^ ((xrow >> 1) & 7)) << 4));
    const int sp = ((p & 1) << 1) | (p >> 1);
    const int d0 = 2 * g, d1 = 2 * g + 1;
    const unsigned wrow = (unsigned)((8 * hh + q4) * 256 + 8 * (sp & 1));
    const unsigned woff0 = wrow + (unsigned)((4 * (d0 ^ q4) + 2 * blk + (sp >> 1)) << 4);
    const unsigned woff1 = wrow + (unsigned)((4 * (d1 ^ q4) + 2 * blk + (sp >> 1)) << 4);
    wait_bar<4>();
    asm volatile("" : "+v"(rq));
    rq += 1u;
    if (w == 7 && lane < 16) st_sc1(ws + WS_REQ + peer_off, rq);
    PFrag f0 = proj_load<0, 0>(lds, xoff, woff0, woff1);
    PFrag f1 = proj_load<0, 1>(lds, xoff, woff0, woff1);
    proj_stage<0>(c, lds, xoff, woff0, woff1, q0, q1, f0, f1);
    proj_stage<1>(c, lds, xoff, woff0, woff1, q0, q1, f0, f1);
    proj_stage<2>(c, lds, xoff, woff0, woff1, q0, q1, f0, f1);
    proj_stage<3>(c, lds, xoff, woff0, woff1, q0, q1, f0, f1);
    proj_stage<4>(c, lds, xoff, woff0, woff1, q0, q1, f0, f1);
    proj_stage<5>(c, lds, xoff, woff0, woff1, q0, q1, f0, f1);
    proj_stage<6>(c, lds, xoff, woff0, woff1, q0, q1, f0, f1);
    proj_stage<7>(c, lds, xoff, woff0, woff1, q0, q1, f0, f1);
    proj_stage<8>(c, lds, xoff, woff0, woff1, q0, q1, f0, f1);
    proj_stage<9>(c, lds, xoff, woff0, woff1, q0, q1, f0, f1);
  }
  bf16x8 qf[8];
  {
    lds_ptr e = lds + XSLOT * SLOT + qt * 8192 + g * 4096 + lane * 16;
    *(__attribute__((address_space(3))) bf16x8*)(e) = pack8<0>(q0);
    *(__attribute__((address_space(3))) bf16x8*)(e + 1024) = pack8<1>(q0);
    *(__attribute__((address_space(3))) bf16x8*)(e + 2048) = pack8<0>(q1);
    *(__attribute__((address_space(3))) bf16x8*)(e + 3072) = pack8<1>(q1);
  }
  lds_bar();
  dma_kv2<4>(c);
  {
    lds_cptr e = lds + XSLOT * SLOT + qt * 8192 + lane * 16;
#pragma unroll
    for (int ks = 0; ks < 8; ++ks) qf[ks] = ldsr128(e + ks * 1024);
  }
  f32x16 o[4] = {};
  SmState st;
  st.l = 0.f;
  float m, l;
  {
    const int krow = 32 * g + r32;
    const unsigned koff = (unsigned)(krow * 256 + ((hh ^ (krow & 15)) << 4));
    const unsigned voff = (unsigned)((32 * g + 4 * hh + q4) * 256 + 8 * (p & 1) + ((4 * q4 + 2 * blk + (p >> 1)) << 4));
    f32x16 sA, sB;
    u32x4 pa0, pa1, pb0, pb1;
    float fsp; bool mvp;
#define KSTEP(N, SN, SO, W0, W1)                                   \
    fsp = st.fs; mvp = st.moved;                                   \
    k_step<N, 2>(c, lds, koff, qf, SN, SO, st, W0, W1);            \
    sm_decide<false>(SN, st);
#define VSTEP(N, P0, P1, SO, W0, W1) v_step<N, 1>(c, lds, voff, P0, P1, fsp, mvp, o, SO, st, W0, W1);
    k_step<0, 0>(c, lds, koff, qf, sA, sA, st, pa0, pa1);
    sm_decide<true>(sA, st);
    fsp = 1.f; mvp = false;
    k_step<1, 3>(c, lds, koff, qf, sB, sA, st, pa0, pa1);
    sm_decide<false>(sB, st);
    VSTEP(2, pa0, pa1, sB, pb0, pb1)   KSTEP(3, sA, sB, pb0, pb1)
    VSTEP(4, pb0, pb1, sA, pa0, pa1)   KSTEP(5, sB, sA, pa0, pa1)
    VSTEP(6, pa0, pa1, sB, pb0, pb1)   KSTEP(7, sA, sB, pb0, pb1)
    VSTEP(8, pb0, pb1, sA, pa0, pa1)   KSTEP(9, sB, sA, pa0, pa1)
    VSTEP(10, pa0, pa1, sB, pb0, pb1)  KSTEP(11, sA, sB, pb0, pb1)
    VSTEP(12, pb0, pb1, sA, pa0, pa1)  KSTEP(13, sB, sA, pa0, pa1)
    {
      const float fs7 = st.fs; const bool mv7 = st.moved;
      v_step<14, 3>(c, lds, voff, pa0, pa1, fsp, mvp, o, sB, st, pb0, pb1);
      m = st.mref;
      l = sum_halves(st.l);
      *(__attribute__((address_space(3))) f32x2*)(lds + 2 * SLOT + w * 512 + lane * 8) = f32x2{m, l};
      v_step<15, 0>(c, lds, voff, pb0, pb1, fs7, mv7, o, sA, st, pa0, pa1);
    }
#undef KSTEP
#undef VSTEP
  }
  {
    const int w2 = w ^ 4;
    const f32x2 ml2 = *(const __attribute__((address_space(3))) f32x2*)(lds + 2 * SLOT + w2 * 512 + lane * 8);
    lds_bar();
    lds_ptr mb = lds + w * 8192 + lane * 16;
    f32x16 sa, sb, ka, kb2;
    if (g == 0) { sa = o[2]; sb = o[3]; ka = o[0]; kb2 = o[1]; }
    else        { sa = o[0]; sb = o[1]; ka = o[2]; kb2 = o[3]; }
#pragma unroll
    for (int i = 0; i < 4; ++i) {
      *(__attribute__((address_space(3))) f32x4*)(mb + i * 1024) = f32x4{sa[4 * i], sa[4 * i + 1], sa[4 * i + 2], sa[4 * i + 3]};
      *(__attribute__((address_space(3))) f32x4*)(mb + 4096 + i * 1024) = f32x4{sb[4 * i], sb[4 * i + 1], sb[4 * i + 2], sb[4 * i + 3]};
    }
    const float mt = fmaxf(m, ml2[0]);
    const float a1 = __builtin_amdgcn_exp2f((m - mt) * SCL), a2 = __builtin_amdgcn_exp2f((ml2[0] - mt) * SCL);
    const float inv = 1.0f / (a1 * l + a2 * ml2[1]);
    const float f1 = a1 * inv, f2 = a2 * inv;
    lds_bar();
    lds_cptr pb = lds + w2 * 8192 + lane * 16;
#pragma unroll
    for (int i = 0; i < 4; ++i) {
      const f32x4 u0 = *(const __attribute__((address_space(3))) f32x4*)(pb + i * 1024);
      const f32x4 u1 = *(const __attribute__((address_space(3))) f32x4*)(pb + 4096 + i * 1024);
#pragma unroll
      for (int j = 0; j < 4; ++j) {
        ka[4 * i + j] = ka[4 * i + j] * f1 + u0[j] * f2;
        kb2[4 * i + j] = kb2[4 * i + j] * f1 + u1[j] * f2;
      }
    }
    const int m16 = 2 * qt + (r32 >> 4);
    if (m16 < 7) {
      char* ob = ws + WS_O + ((size_t)((b * 128 + h * 4 + 2 * g) * 7 + m16)) * 1024 + (16 * hh + (r32 & 15)) * 16;
      auto pk = [](const f32x16& v, int u) {
        u32x4 t = {cvtpk(v[4 * u], v[4 * u + 1]), cvtpk(v[4 * u + 2], v[4 * u + 3]), cvtpk(v[4 * u + 8], v[4 * u + 9]),
                   cvtpk(v[4 * u + 10], v[4 * u + 11])};
        return __builtin_bit_cast(bf16x8, t);
      };
      store_wt(ob, pk(ka, 0));
      store_wt(ob + 512, pk(ka, 1));
      store_wt(ob + 7 * 1024, pk(kb2, 0));
      store_wt(ob + 7 * 1024 + 512, pk(kb2, 1));
    }
  }
  unsigned rv = 0;
  if (w == 7) rv = ld_sc1(ws + WS_REQ + my_off);
  wait_bar<0>();
  if (w == 7) rv = handoff_wait<0, 8>(ws, lane, my_off, peer_off, rq, rv);
  wait_bar<0>();
  const int gg = lane >> 4;
  const auto prs = __builtin_amdgcn_make_buffer_rsrc((void*)(ws + WS_PART + (size_t)(bp * 8 + nt) * 8 * 14 * 2560), 0,
                                                     8 * 14 * 2560, 0x00020000);
  f32x4 acc[2][5];
#pragma unroll
  for (int i = 0; i < 2; ++i)
#pragma unroll
    for (int j = 0; j < 5; ++j) acc[i][j] = f32x4{0.f, 0.f, 0.f, 0.f};
  if (w < 7) {
    const auto ors = __builtin_amdgcn_make_buffer_rsrc((void*)(ws + WS_O + (size_t)(2 * bp * 128 + 16 * k8) * 7 * 1024), 0,
                                                       (128 + 16) * 7 * 1024, 0x00020000);
    const int mo = w * 1024 + lane * 16;
    const unsigned wl = (unsigned)(WO1 + (4 * gg + q4) * 160 + 8 * p);
    constexpr int PD = 4;
    const int rot = 2 * nt;
    u32x4 of[PD][2];
#pragma unroll
    for (int ks = 0; ks < PD; ++ks) {
      of[ks][0] = __builtin_amdgcn_raw_buffer_load_b128(ors, mo, ((ks + rot) & 15) * 7168, 16);
      of[ks][1] = __builtin_amdgcn_raw_buffer_load_b128(ors, mo, (128 + ((ks + rot) & 15)) * 7168, 16);
    }
    SBAR();
#pragma unroll
    for (int ks = 0; ks < 16; ++ks) {
      lds_cptr wp = lds + (wl + (unsigned)(((ks + rot) & 15) * 5120));
      bf16x8 wf[5];
#pragma unroll
      for (int j = 0; j < 5; ++j) wf[j] = cat8(vtr(wp + 32 * j), vtr(wp + 32 * j + 2560));
      const bf16x8 o0 = __builtin_bit_cast(bf16x8, of[ks % PD][0]), o1 = __builtin_bit_cast(bf16x8, of[ks % PD][1]);
#pragma unroll
      for (int j = 0; j < 5; ++j) {
        acc[0][j] = MFMA16(wf[j], o0, acc[0][j]);
        acc[1][j] = MFMA16(wf[j], o1, acc[1][j]);
      }
      if (ks + PD < 16) {
        of[ks % PD][0] = __builtin_amdgcn_raw_buffer_load_b128(ors, mo, ((ks + PD + rot) & 15) * 7168, 16);
        of[ks % PD][1] = __builtin_amdgcn_raw_buffer_load_b128(ors, mo, (128 + ((ks + PD + rot) & 15)) * 7168, 16);
      }
      SBAR();
    }
#pragma unroll
    for (int i = 0; i < 2; ++i) {
      if (!(gg == (k8 & 3) && i == (k8 >> 2))) {
        const int po = (k8 * 14 + i * 7 + w) * 2560;
        const u32x4 ra = {cvtpk(acc[i][0][0], acc[i][0][1]), cvtpk(acc[i][0][2], acc[i][0][3]),
                          cvtpk(acc[i][1][0], acc[i][1][1]), cvtpk(acc[i][1][2], acc[i][1][3])};
        const u32x4 rb = {cvtpk(acc[i][2][0], acc[i][2][1]), cvtpk(acc[i][2][2], acc[i][2][3]),
                          cvtpk(acc[i][3][0], acc[i][3][1]), cvtpk(acc[i][3][2], acc[i][3][3])};
        const u32x2 rc = {cvtpk(acc[i][4][0], acc[i][4][1]), cvtpk(acc[i][4][2], acc[i][4][3])};
        __builtin_amdgcn_raw_buffer_store_b128(ra, prs, po + lane * 16, 0, 16);
        __builtin_amdgcn_raw_buffer_store_b128(rb, prs, po + 1024 + lane * 16, 0, 16);
        __builtin_amdgcn_raw_buffer_store_b64(rc, prs, po + 2048 + lane * 8, 0, 16);
      }
    }
  } else {
    handoff_finish<0, 8>(ws, lane, my_off, peer_off, rv);
    rv = ld_sc1(ws + WS_REQ + my_off);
  }
  wait_bar<0>();
  if (w == 7) rv = handoff_wait<8, 8>(ws, lane, my_off, peer_off, rq, rv);
  wait_bar<0>();
  if (w == 7) handoff_finish<8, 8>(ws, lane, my_off, peer_off, rv);
  if (w < 7) {
    const int ih = k8 >> 2;
    const int sl2 = 16 * (k8 & 3) + (lane & 15);
    u32x4 ua[2], ub[2];
    u32x2 uc[2];
#pragma unroll
    for (int q = 0; q < 2; ++q) {
      const int po = ((gg + 4 * q) * 14 + ih * 7 + w) * 2560;
      ua[q] = __builtin_amdgcn_raw_buffer_load_b128(prs, po + sl2 * 16, 0, 16);
      ub[q] = __builtin_amdgcn_raw_buffer_load_b128(prs, po + 1024 + sl2 * 16, 0, 16);
      uc[q] = __builtin_amdgcn_raw_buffer_load_b64(prs, po + 2048 + sl2 * 8, 0, 16);
    }
    auto unpk = [](unsigned lo, unsigned hi) {
      return f32x4{__uint_as_float(lo << 16), __uint_as_float(lo & 0xffff0000u), __uint_as_float(hi << 16),
                   __uint_as_float(hi & 0xffff0000u)};
    };
    f32x4 r[5];
#pragma unroll
    for (int j = 0; j < 5; ++j) {
      const f32x4 own = ih ? acc[1][j] : acc[0][j];
      f32x4 v2[2];
#pragma unroll
      for (int q = 0; q < 2; ++q) {
        const f32x4 pv = j == 0 ? unpk(ua[q][0], ua[q][1]) : j == 1 ? unpk(ua[q][2], ua[q][3])
                       : j == 2 ? unpk(ub[q][0], ub[q][1]) : j == 3 ? unpk(ub[q][2], ub[q][3]) : unpk(uc[q][0], uc[q][1]);
        v2[q] = gg + 4 * q == k8 ? own : pv;
      }
      f32x4 t = v2[0] + v2[1];
#pragma unroll
      for (int e = 0; e < 4; ++e) {
        t[e] += __shfl_xor(t[e], 16);
        t[e] += __shfl_xor(t[e], 32);
      }
      r[j] = t;
    }
    const f32x4 t0 = gg == 0 ? r[0] : gg == 1 ? r[1] : gg == 2 ? r[2] : r[3];
    unsigned short* op = out + (size_t)((2 * bp + ih) * SQ + 16 * w + (lane & 15)) * DM + 80 * nt + 4 * (k8 & 3);
    *(u32x2*)(op + 16 * gg) = u32x2{cvtpk(t0[0], t0[1]), cvtpk(t0[2], t0[3])};
    if (gg == 0) *(u32x2*)(op + 64) = u32x2{cvtpk(r[4][0], r[4][1]), cvtpk(r[4][2], r[4][3])};
  }
}

}

extern "C" void kernel_launch(void* const* d_in, const int* in_sizes, int n_in, void* d_out, int out_size, void* d_ws,
                              size_t ws_size, hipStream_t stream) {
  (void)in_sizes; (void)n_in; (void)out_size; (void)ws_size;
  (void)hipFuncSetAttribute((const void*)gqa_fused, hipFuncAttributeMaxDynamicSharedMemorySize, LDS1);
  hipLaunchKernelGGL(gqa_fused, dim3(256), dim3(512), LDS1, stream, (const char*)d_in[0], (const char*)d_in[1],
                     (const char*)d_in[2], (const char*)d_in[3], (const char*)d_in[4], (char*)d_ws,
                     (unsigned short*)d_out);
}
```
